```python
import torch

def _softmax(x, axis=-1):
    return torch.softmax(x, dim=axis)
def _gelu(x):
    return torch.nn.functional.gelu(x, approximate="tanh")
def _silu(x):
    return torch.nn.functional.silu(x)
def _sigmoid(x):
    return torch.sigmoid(x)
def _layernorm(x, axis=-1, eps=1e-5):
    m = x.mean(dim=axis, keepdim=True)
    v = x.var(dim=axis, keepdim=True, unbiased=False)
    return (x - m) / torch.sqrt(v + eps)
def _scatter_add(tgt, idx, src, axis=0):
    return tgt.clone().index_add_(axis, idx, src)
def _segment_sum(src, seg, n):
    out = torch.zeros((n,) + src.shape[1:], dtype=src.dtype)
    return out.index_add_(0, seg, src)
def _pad(x, pw):
    flat = tuple(v for pair in reversed(pw) for v in pair)
    return torch.nn.functional.pad(x, flat)

kernel_name = 'concat_heads__ssm_conv_s4__linear_attn_block__prompt'

def setup_inputs():

    _w = (lambda a: (lambda t: t.to(torch.bfloat16) if t.is_floating_point() else t)(torch.as_tensor(a)))
    def _A_setup():
        B, L, D, N = 1, 2048, 448, 256
        A_re = -(lambda _x: torch.exp(torch.as_tensor(_x)))((lambda *s: torch.randn(*s, dtype=torch.bfloat16) * 3.2)(D, N))
        A_im = torch.pi * (lambda *s: torch.randn(*s, dtype=torch.bfloat16) * 0.05)(D, N)
        B_re = (lambda *s: torch.randn(*s, dtype=torch.bfloat16) * 0.2)(D, N)
        B_im = (lambda *s: torch.randn(*s, dtype=torch.bfloat16) * 0.2)(D, N)
        C_re = (lambda *s: torch.randn(*s, dtype=torch.bfloat16) * 0.2)(D, N)
        C_im = (lambda *s: torch.randn(*s, dtype=torch.bfloat16) * 0.2)(D, N)
        log_dt = (lambda *s: torch.randn(*s, dtype=torch.bfloat16) * 0.05)(D) * 0.5 - 4.0
        Dskip = (lambda *s: torch.randn(*s, dtype=torch.bfloat16) * 0.05)(D)
        return {
            "u": _w((lambda *s: torch.randn(*s, dtype=torch.bfloat16) * 0.05)(B, L, D)),
            "A_re": _w(A_re),
            "A_im": _w(A_im),
            "B_re": _w(B_re),
            "B_im": _w(B_im),
            "C_re": _w(C_re),
            "C_im": _w(C_im),
            "log_dt": _w(log_dt),
            "Dskip": _w(Dskip),
        }
    _A_in = _A_setup()

    def _B_setup():
        B, S, D = 1, 2048, 448
        return {
            "x": _w((lambda *s: torch.randn(*s, dtype=torch.bfloat16) * 0.05)(B, S, D)),
            "wqkv": _w((lambda *s: torch.randn(*s, dtype=torch.bfloat16) * 0.8)(D, 3 * D)),
            "wo": _w((lambda *s: torch.randn(*s, dtype=torch.bfloat16) * 0.1)(D, D)),
        }
    _B_in = _B_setup()

    return {
        "A_u": _A_in["u"],
        "A_A_re": _A_in["A_re"],
        "A_A_im": _A_in["A_im"],
        "A_B_re": _A_in["B_re"],
        "A_B_im": _A_in["B_im"],
        "A_C_re": _A_in["C_re"],
        "A_C_im": _A_in["C_im"],
        "A_log_dt": _A_in["log_dt"],
        "A_Dskip": _A_in["Dskip"],
        "B_x": _B_in["x"],
        "B_wqkv": _B_in["wqkv"],
        "B_wo": _B_in["wo"],
    }

def reference(A_u, A_A_re, A_A_im, A_B_re, A_B_im, A_C_re, A_C_im, A_log_dt, A_Dskip, B_x, B_wqkv, B_wo):
    def _A_ref(u, A_re, A_im, B_re, B_im, C_re, C_im, log_dt, Dskip):
        Bsz, L, D = u.shape
        dt = torch.exp(log_dt)[:, None]
        A = A_re + 1j * A_im
        Bc = B_re + 1j * B_im
        Cc = C_re + 1j * C_im
        logdA = dt * A
        dB = dt * Bc
        l = (lambda x, dt: x.to(dt))(torch.arange(L), A_re.dtype)
        P = torch.exp(logdA[:, :, None] * l[None, None, :])
        K = torch.einsum("dn,dnl,dn->dl", Cc, P, dB).real
        L2 = 2 * L
        Kp = _pad(K, ((0, 0), (0, L)))
        up = (lambda x, *d: x.permute(*d))(u, 0, 2, 1)
        up = _pad(up, ((0, 0), (0, 0), (0, L)))
        Kf = (lambda x, axes=(-2,-1): torch.fft.rfft2(x.float(), dim=axes))(Kp[:, None, :], axes=(-2, -1))
        Uf = (lambda x, axes=(-2,-1): torch.fft.rfft2(x.float(), dim=axes))(up[:, :, None, :], axes=(-2, -1))
        Yf = Uf * Kf[None, :, :, :]
        y = (lambda x, s, axes=(-2,-1): torch.fft.irfft2(x, s=s, dim=axes).to(torch.bfloat16))(Yf, (1, L2), axes=(-2, -1))[:, :, 0, :L]
        y = (lambda x, *d: x.permute(*d))(y, 0, 2, 1) + u * Dskip[None, None, :]
        return y

    def _B_ref(x, wqkv, wo):
        B, S, D = x.shape
        H = 16; Dh = D // H
        qkv = (lambda x, *d: x.permute(*d))((x @ wqkv).reshape(B, S, 3, H, Dh), 2, 0, 3, 1, 4)
        q, k, v = qkv[0], qkv[1], qkv[2]
        phi = lambda t: torch.where(t > 0, t + 1, torch.exp(t)) + 1e-6
        pq, pk = phi(q), phi(k)
        kv = torch.einsum("bhsd,bhse->bhde", pk, v)
        z = torch.einsum("bhsd->bhd", pk)
        num = torch.einsum("bhqd,bhde->bhqe", pq, kv)
        den = torch.einsum("bhqd,bhd->bhq", pq, z)[..., None] + 1e-6
        o = num / den
        return (lambda x, *d: x.permute(*d))(o, 0, 2, 1, 3).reshape(B, S, D) @ wo

    a = _A_ref(A_u, A_A_re, A_A_im, A_B_re, A_B_im, A_C_re, A_C_im, A_log_dt, A_Dskip)
    b = _B_ref(B_x, B_wqkv, B_wo)
    return torch.concatenate([a, b], axis=-1)
```

```cpp
#include <hip/hip_runtime.h>
#include <hip/hip_bf16.h>
#include <stdint.h>
typedef unsigned short u16;
__device__ __forceinline__ float bf2f(u16 v){ return __uint_as_float(((unsigned)v)<<16); }
__device__ __forceinline__ u16 f2bf(float f){ unsigned u=__float_as_uint(f); u += 0x7fff + ((u>>16)&1); return (u16)(u>>16); }
__device__ __forceinline__ float rbf(float f){ return bf2f(f2bf(f)); }
#define L 2048
#define D 448
#define N 256
__global__ void k_kgen(const u16* Are,const u16* Aim,const u16* Bre,const u16* Bim,const u16* Cre,const u16* Cim,const u16* ldt,float* K){
  int d=blockIdx.x; __shared__ float sa[N],sb[N],swr[N],swi[N];
  float dt=rbf(expf(bf2f(ldt[d])));
  for(int n=threadIdx.x;n<N;n+=blockDim.x){int i=d*N+n; sa[n]=dt*bf2f(Are[i]); sb[n]=dt*bf2f(Aim[i]);
    float br=dt*bf2f(Bre[i]),bi=dt*bf2f(Bim[i]),cr=bf2f(Cre[i]),ci=bf2f(Cim[i]); swr[n]=cr*br-ci*bi; swi[n]=cr*bi+ci*br;}
  __syncthreads();
  for(int l=threadIdx.x;l<L;l+=blockDim.x){ float lf=rbf((float)l); float acc=0;
    for(int n=0;n<N;n++){ float e=__expf(sa[n]*lf); float s,c; __sincosf(sb[n]*lf,&s,&c); acc+=e*(swr[n]*c-swi[n]*s);} K[d*L+l]=acc; }
}
__global__ void k_conv(const u16* u,const float* K,const u16* Dsk,u16* out){
  int d=blockIdx.x; __shared__ float su[L],sk[L];
  for(int l=threadIdx.x;l<L;l+=blockDim.x){su[l]=bf2f(u[l*D+d]); sk[l]=K[d*L+l];}
  __syncthreads();
  for(int t=threadIdx.x;t<L;t+=blockDim.x){float acc=0; for(int j=0;j<=t;j++) acc+=sk[j]*su[t-j];
    out[t*896+d]=f2bf(rbf(acc)+rbf(su[t]*bf2f(Dsk[d])));}
}
__global__ void k_qkv(const u16* x,const u16* w,float* qkv){
  int s=blockIdx.x; __shared__ float sx[D];
  for(int i=threadIdx.x;i<D;i+=blockDim.x) sx[i]=bf2f(x[s*D+i]);
  __syncthreads();
  for(int c=threadIdx.x;c<3*D;c+=blockDim.x){float acc=0; for(int k=0;k<D;k++) acc+=sx[k]*bf2f(w[k*3*D+c]);
    acc=rbf(acc); if(c<2*D){ acc = acc>0? rbf(acc+1.f): rbf(expf(acc)); acc=rbf(acc+1e-6f);} qkv[s*3*D+c]=acc;}
}
__global__ void k_kv(const float* qkv,float* kv){
  int h=blockIdx.x/28, dd=blockIdx.x%28; int e=threadIdx.x; if(e>28) return;
  float acc=0; for(int s=0;s<L;s++){ float k=qkv[s*3*D+D+h*28+dd]; float v=e<28?qkv[s*3*D+2*D+h*28+e]:1.f; acc+=k*v;}
  kv[(h*28+dd)*29+e]=rbf(acc);
}
__global__ void k_out(const float* qkv,const float* kv,const u16* wo,u16* out){
  int s=blockIdx.x; __shared__ float so[D];
  for(int c=threadIdx.x;c<D;c+=blockDim.x){int h=c/28,e=c%28; float num=0,den=0;
    for(int dd=0;dd<28;dd++){float q=qkv[s*3*D+h*28+dd]; num+=q*kv[(h*28+dd)*29+e]; den+=q*kv[(h*28+dd)*29+28];}
    so[c]=rbf(rbf(num)/rbf(rbf(den)+1e-6f));}
  __syncthreads();
  for(int c=threadIdx.x;c<D;c+=blockDim.x){float acc=0; for(int k=0;k<D;k++) acc+=so[k]*bf2f(wo[k*D+c]); out[s*896+448+c]=f2bf(acc);}
}
extern "C" void kernel_launch(void* const* d_in, const int* in_sizes, int n_in,
                              void* d_out, int out_size, void* d_ws, size_t ws_size,
                              hipStream_t stream) {
  const u16* const* in=(const u16* const*)d_in;
  float* K=(float*)d_ws; float* qkv=K+D*L; float* kv=qkv+L*3*D;
  k_kgen<<<D,256,0,stream>>>(in[1],in[2],in[3],in[4],in[5],in[6],in[7],K);
  k_conv<<<D,256,0,stream>>>(in[0],K,in[8],(u16*)d_out);
  k_qkv<<<L,256,0,stream>>>(in[9],in[10],qkv);
  k_kv<<<16*28,64,0,stream>>>(qkv,kv);
  k_out<<<L,256,0,stream>>>(qkv,kv,in[11],(u16*)d_out);
}
```

```cpp
#pragma clang diagnostic ignored "-Wint-to-pointer-cast"
#include <hip/hip_runtime.h>
#include <hip/hip_bf16.h>
#include <stdint.h>

typedef unsigned short u16;
typedef unsigned int u32;
typedef unsigned long long u64;
typedef __attribute__((ext_vector_type(2))) float f2;
typedef __attribute__((ext_vector_type(4))) float f4;
typedef __attribute__((ext_vector_type(16))) float f16v;
typedef __attribute__((ext_vector_type(2))) u32 u2;
typedef __attribute__((ext_vector_type(4))) u32 u4;
typedef __attribute__((ext_vector_type(8))) __bf16 bf8v;
typedef __attribute__((ext_vector_type(2))) __bf16 bf2v;
typedef __attribute__((ext_vector_type(4))) short s4v;
#define LAS __attribute__((address_space(3)))
#define GAS __attribute__((address_space(1)))
#define DI __device__ __forceinline__

constexpr u32 WS_FLAG = 1024;
constexpr u32 WS_EX   = 8192;
constexpr u32 EX_KVF  = 0;
constexpr u32 EX_PQ   = 32768;
constexpr u32 EX_PART = EX_PQ + 1835008;
constexpr u32 EX_SIZE = EX_PART + 112 * 44 * 512;
constexpr u32 SENT = 0xFFFFFFFFu;
constexpr int SPIN_CAP = 1 << 17;

#define STAMP(i) do {} while (0)

DI u32 pk(float lo, float hi) { f2 v = {lo, hi}; return __builtin_bit_cast(u32, __builtin_convertvector(v, bf2v)); }
DI float bflo(u32 v) { return __uint_as_float(v << 16); }
DI float bfhi(u32 v) { return __uint_as_float(v & 0xffff0000u); }
DI f4 mfma16(u4 a, u4 b, f4 c) { return __builtin_amdgcn_mfma_f32_16x16x32_bf16(__builtin_bit_cast(bf8v, a), __builtin_bit_cast(bf8v, b), c, 0, 0, 0); }
DI f16v mfma32(u4 a, u4 b, f16v c) { return __builtin_amdgcn_mfma_f32_32x32x16_bf16(__builtin_bit_cast(bf8v, a), __builtin_bit_cast(bf8v, b), c, 0, 0, 0); }
DI f4 mfma16k16(u2 a, u2 b, f4 c) { return __builtin_amdgcn_mfma_f32_16x16x16bf16_1k(__builtin_bit_cast(s4v, a), __builtin_bit_cast(s4v, b), c, 0, 0, 0); }
DI u4 lds128(u32 a) { return *(LAS u4*)a; }
DI u2 lds64(u32 a) { return *(LAS u2*)a; }
DI u2 ldstr(u32 a) { return __builtin_bit_cast(u2, __builtin_amdgcn_ds_read_tr16_b64_v4i16((LAS s4v*)a)); }
DI void sts128(u32 a, u4 v) { *(LAS u4*)a = v; }
DI void sts64(u32 a, u2 v) { *(LAS u2*)a = v; }
DI void sts32(u32 a, u32 v) { *(LAS u32*)a = v; }
DI void bar() { asm volatile("s_waitcnt lgkmcnt(0)" ::: "memory"); __builtin_amdgcn_s_barrier(); asm volatile("" ::: "memory"); }
#define WAITV(n) asm volatile("s_waitcnt vmcnt(" #n ")" ::: "memory")
DI void dma16(const char* sbase, u32 voff, u32 ldst) {
    u32 keep;
    asm volatile("s_mov_b32 %0, m0\n\ts_mov_b32 m0, %3\n\ts_nop 2\n\tglobal_load_lds_dwordx4 %1, %2\n\ts_mov_b32 m0, %0"
                 : "=&s"(keep) : "v"(voff), "s"(sbase), "s"(ldst) : "memory");
}
DI void dma16c(const char* sbase, u32 voff, u32 ldst) {
    u32 keep;
    asm volatile("s_mov_b32 %0, m0\n\ts_mov_b32 m0, %3\n\ts_nop 2\n\tglobal_load_lds_dwordx4 %1, %2 sc1\n\ts_mov_b32 m0, %0"
                 : "=&s"(keep) : "v"(voff), "s"(sbase), "s"(ldst) : "memory");
}
DI u32 san(u32 v) { return v < 0xFFFFFFFEu ? v : 0xFFFFFFFEu; }
DI u32 max3u(u32 a, u32 b, u32 c) { u32 m = a > b ? a : b; return m > c ? m : c; }
DI u32 max4u(u32 m, u4 v) { return max3u(max3u(m, v.x, v.y), v.z, v.w); }
DI f2 cmul(f2 a, f2 b, f2 bp) { f2 t = a.xx * b; return __builtin_elementwise_fma(a.yy, bp, t); }
DI f2 cexp(float a, float th) {
    float m = __builtin_amdgcn_exp2f(a), fr = __builtin_amdgcn_fractf(th);
    f2 r = {__builtin_amdgcn_cosf(fr), __builtin_amdgcn_sinf(fr)};
    return r * m;
}
DI float phi1(float a1) {
    const float L2E = 1.4426950408889634f;
    float e = __builtin_amdgcn_fmed3f(__builtin_amdgcn_exp2f(__builtin_fmaf(a1, L2E, -L2E)), 0.f, 1.f);
    return __builtin_fmaxf(a1, e) + 1e-6f;
}

struct Params {
    const u16 *u, *Are, *Aim, *Bre, *Bim, *Cre, *Cim, *ldt, *Dsk, *x, *wqkv, *wo;
    u16* out;
    char* ws;
};

template <int T0, int T1>
DI void conv_phase(u32 ua, u32 ub, u32 ka, u32 ya) {
    f4 acc[T1 - T0];
#pragma unroll
    for (int i = 0; i < T1 - T0; i++) acc[i] = f4{0.f, 0.f, 0.f, 0.f};
    u4 U[2][T1], Kf[2][T1];
    auto load = [&](int b) {
#pragma unroll
        for (int p = 0; p < T1; p++) {
            u2 lo = lds64(ua + 512 * p), hi = lds64(ub + 512 * p);
            U[b][p] = u4{lo.x, lo.y, hi.x, hi.y};
            Kf[b][p] = lds128(ka + 512 * p);
        }
        ua += 64; ub += 64; ka -= 64;
    };
    auto comp = [&](int b) {
#pragma unroll
        for (int p = 0; p < T1; p++)
#pragma unroll
            for (int i = T0; i < T1; i++)
                if (p <= i) acc[i - T0] = mfma16(U[b][p], Kf[b][i - p], acc[i - T0]);
    };
    load(0);
#pragma unroll 1
    for (int r = 0; r < 3; r++) { load(1); comp(0); load(0); comp(1); }
    load(1); comp(0); comp(1);
#pragma unroll
    for (int i = T0; i < T1; i++) sts64(ya + 512 * i, u2{pk(acc[i - T0][0], acc[i - T0][1]), pk(acc[i - T0][2], acc[i - T0][3])});
}

__global__ __launch_bounds__(512, 2) void fused_kernel(Params P) {
    __shared__ __attribute__((aligned(16))) char smem[163840];
    const u32 sb = (u32)(size_t)(LAS char*)smem;
    const int tid = threadIdx.x, lane = tid & 63;
    const int w = __builtin_amdgcn_readfirstlane(tid >> 6);
    const int b = blockIdx.x, xcd = b & 7, idx = b >> 3;
    STAMP(0);
    const u64 E = *(const __attribute__((address_space(4))) u64*)P.ws;
    const u64 tag = E * 6364136223846793005ull + 1442695040888963407ull;
    GAS u64* flags = (GAS u64*)(P.ws + WS_FLAG);
    const __amdgpu_buffer_rsrc_t rex = __builtin_amdgcn_make_buffer_rsrc(P.ws + WS_EX, 0, EX_SIZE, 0x00020000);

    if (idx < 14) {
        const int cg = xcd * 14 + idx, d0 = cg * 4;
        const int po = d0 * 128 + tid;
        const u32 pAr = ((const u32*)P.Are)[po], pAi = ((const u32*)P.Aim)[po], pBr = ((const u32*)P.Bre)[po],
                  pBi = ((const u32*)P.Bim)[po], pCr = ((const u32*)P.Cre)[po], pCi = ((const u32*)P.Cim)[po];
        const u32 ldtv = P.ldt[d0 + (w >> 1)];
        u2 R[2][4], Rp[2];
        if (w >= 4) {
            const int h = tid - 256;
#pragma unroll
            for (int g = 0; g < 2; g++) {
                const char* up = (const char*)P.u + (size_t)(1024 * g + 4 * h) * 896 + d0 * 2;
#pragma unroll
                for (int i = 0; i < 4; i++) R[g][i] = *(const u2*)(up + i * 896);
                Rp[g] = u2{0u, 0u};
                if (lane == 0 && (g | h)) Rp[g] = *(const u2*)(up - 896);
            }
        }
        const float dt = bflo(pk(expf(bflo(ldtv)), 0.f));
        const float L2E = 1.4426950408889634f, I2PI = 0.15915494309189535f;
        const u32 scr = sb + w * 16896;
        const u32 wa = scr + lane * 8;
        float ar[2], th[2];
        f2 Wc[2];
        {
            ar[0] = dt * bflo(pAr) * L2E; ar[1] = dt * bfhi(pAr) * L2E;
            th[0] = dt * bflo(pAi) * I2PI; th[1] = dt * bfhi(pAi) * I2PI;
            float br0 = dt * bflo(pBr), bi0 = dt * bflo(pBi), br1 = dt * bfhi(pBr), bi1 = dt * bfhi(pBi);
            Wc[0] = f2{bflo(pCr) * br0 - bflo(pCi) * bi0, -(bflo(pCr) * bi0 + bflo(pCi) * br0)};
            Wc[1] = f2{bfhi(pCr) * br1 - bfhi(pCi) * bi1, -(bfhi(pCr) * bi1 + bfhi(pCi) * br1)};
        }
        STAMP(1);
        {
            f2 z0 = cexp(ar[0] * 8.f, th[0] * 8.f), z1 = cexp(ar[1] * 8.f, th[1] * 8.f);
            f2 z0p = {-z0.y, z0.x}, z1p = {-z1.y, z1.x};
            f2 e0 = z0, e1 = z1;
            sts64(wa, u2{0x00003F80u, 0x00003F80u});
#pragma unroll
            for (int c = 1; c < 32; c++) {
                sts64(wa + c * 528, u2{pk(e0.x, e0.y), pk(e1.x, e1.y)});
                if (c < 31) { e0 = cmul(e0, z0, z0p); e1 = cmul(e1, z1, z1p); }
            }
        }
        const u32 ra = scr + (lane & 31) * 528 + (lane >> 5) * 16;
        u4 Bf[16];
#pragma unroll
        for (int s = 0; s < 16; s++) Bf[s] = lds128(ra + 32 * s);
        if (w < 4) {
            const u4 sv = {SENT, SENT, SENT, SENT};
#pragma unroll
            for (int i = 0; i < 10; i++) __builtin_amdgcn_raw_buffer_store_b128(sv, rex, (cg * 4 + w + 448 * i) * 1024 + lane * 16, 0, 16);
        }
        asm volatile("" ::: "memory");
        {
            f2 y0 = cexp(ar[0], -th[0]), y1 = cexp(ar[1], -th[1]);
            f2 g0 = cexp(ar[0] * 256.f, -th[0] * 256.f), g1 = cexp(ar[1] * 256.f, -th[1] * 256.f);
            f2 y0p = {-y0.y, y0.x}, y1p = {-y1.y, y1.x}, g0p = {-g0.y, g0.x}, g1p = {-g1.y, g1.x};
            f2 p0[8], p1[8];
            p0[0] = Wc[0]; p1[0] = Wc[1];
#pragma unroll
            for (int a = 1; a < 8; a++) { p0[a] = cmul(p0[a - 1], y0, y0p); p1[a] = cmul(p1[a - 1], y1, y1p); }
            auto put = [&](int row, f2 v0, f2 v1) { sts64(wa + row * 528, u2{pk(v0.x, v0.y), pk(v1.x, v1.y)}); };
#pragma unroll
            for (int a = 0; a < 8; a++) put(a, p0[a], p1[a]);
#pragma unroll
            for (int a = 0; a < 4; a++) { p0[2 * a] = cmul(p0[2 * a], g0, g0p); p1[2 * a] = cmul(p1[2 * a], g1, g1p); put(8 + a, p0[2 * a], p1[2 * a]); }
#pragma unroll
            for (int k = 0; k < 2; k++)
#pragma unroll
                for (int a = 0; a < 2; a++) { p0[4 * a] = cmul(p0[4 * a], g0, g0p); p1[4 * a] = cmul(p1[4 * a], g1, g1p); put(12 + 2 * k + a, p0[4 * a], p1[4 * a]); }
#pragma unroll
            for (int k = 0; k < 5; k++) { p0[0] = cmul(p0[0], g0, g0p); p1[0] = cmul(p1[0], g1, g1p); put(16 + k, p0[0], p1[0]); }
        }
        f16v kc;
#pragma unroll
        for (int i = 0; i < 16; i++) kc[i] = 0.f;
#pragma unroll
        for (int s = 0; s < 16; s++) kc = mfma32(lds128(ra + 32 * s), Bf[s], kc);
        {
            const u32 tb = sb + 135168 + w * 3072;
            const int c = lane & 31, hh = lane >> 5;
            sts128(tb + (8 * c + 4 * hh) * 4, __builtin_bit_cast(u4, f4{kc[0], kc[1], kc[2], kc[3]}));
            const u32 a1 = hh ? 384 + 2 * c : 256 + 4 * c, a2 = hh ? 448 + 2 * c : 258 + 4 * c;
            sts64(tb + a1 * 4, __builtin_bit_cast(u2, f2{kc[4], kc[5]}));
            sts64(tb + a2 * 4, __builtin_bit_cast(u2, f2{kc[6], kc[7]}));
#pragma unroll
            for (int i = 0; i < 4; i++) sts32(tb + (512 + 128 * hh + c + 32 * i) * 4, __float_as_uint(kc[8 + i]));
        }
        if (w < 4) {
            WAITV(0);
            if (lane == 0) __hip_atomic_store(flags + cg * 4 + w, tag, __ATOMIC_RELAXED, __HIP_MEMORY_SCOPE_AGENT);
        }
        STAMP(2);
        bar();
        constexpr u32 UB = 0, KB = 66560, YB = 84992;
        if (w < 4) {
            const u32 t0 = sb + 135168 + w * 6144, t1 = t0 + 3072, kb = sb + KB + w * 4608 + 512;
            const float dsk = bflo(P.Dsk[d0 + w]);
            {
                const bool s1 = lane >= 32;
                const u32 i0 = (s1 ? 128 + 4 * lane : 8 * lane) * 4;
                f4 a = __builtin_bit_cast(f4, lds128(t0 + i0)) + __builtin_bit_cast(f4, lds128(t1 + i0));
                f4 c = __builtin_bit_cast(f4, lds128(t0 + i0 + 16)) + __builtin_bit_cast(f4, lds128(t1 + i0 + 16));
                if (lane == 0) a[0] += dsk;
                sts128(kb + lane * 16, u4{pk(s1 ? c[0] : c[3], s1 ? a[3] : c[2]), pk(s1 ? a[2] : c[1], s1 ? a[2] : c[0]),
                                          pk(s1 ? a[2] : a[3], s1 ? a[1] : a[2]), pk(s1 ? a[0] : a[1], a[0])});
            }
            {
                const u32 i0 = (384 + 2 * lane) * 4;
                f2 a = __builtin_bit_cast(f2, lds64(t0 + i0)) + __builtin_bit_cast(f2, lds64(t1 + i0));
                float c = *(LAS float*)(t0 + i0 + 8) + *(LAS float*)(t1 + i0 + 8);
                sts128(kb + (64 + lane) * 16, u4{pk(c, c), pk(a[1], a[1]), pk(a[1], a[0]), pk(a[0], a[0])});
            }
#pragma unroll
            for (int k = 0; k < 2; k++) {
                const u32 i0 = (512 + 64 * k + lane) * 4;
                float a = *(LAS float*)(t0 + i0) + *(LAS float*)(t1 + i0), c = *(LAS float*)(t0 + i0 + 4) + *(LAS float*)(t1 + i0 + 4);
                const u32 aa = pk(a, a);
                sts128(kb + (128 + 64 * k + lane) * 16, u4{pk(c, c), pk(c, (lane & 1) ? c : a), aa, aa});
            }
            if (lane < 32) sts128(kb - 512 + lane * 16, u4{0u, 0u, 0u, 0u});
            if (lane < 32) {
                const int cp = lane >> 3, dw = lane & 7;
                if (dw < 8 - ((cp + 1) >> 1)) sts32(sb + UB + w * 16640 + cp * 4160 + dw * 4, 0u);
            }
        } else {
            const int h = tid - 256;
#pragma unroll
            for (int g = 0; g < 2; g++) {
                u2 pv;
                pv.x = __builtin_amdgcn_update_dpp(Rp[g].x, R[g][3].x, 0x138, 0xf, 0xf, false);
                pv.y = __builtin_amdgcn_update_dpp(Rp[g].y, R[g][3].y, 0x138, 0xf, 0xf, false);
                const u32 yb = sb + UB + (1024 * g + 4 * h + 16) * 2;
#pragma unroll
                for (int ch = 0; ch < 4; ch++) {
                    const u32 sel = (ch & 1) ? 0x07060302u : 0x05040100u;
                    const int c2 = ch >> 1;
                    const u32 e01 = __builtin_amdgcn_perm(R[g][1][c2], R[g][0][c2], sel), e23 = __builtin_amdgcn_perm(R[g][3][c2], R[g][2][c2], sel);
                    const u32 om0 = __builtin_amdgcn_perm(R[g][0][c2], pv[c2], sel), o12 = __builtin_amdgcn_perm(R[g][2][c2], R[g][1][c2], sel);
                    const u32 cb = yb + ch * 16640;
                    sts64(cb, u2{e01, e23});
                    sts32(cb + 4160 - 4, om0); sts32(cb + 4160, o12);
                    sts32(cb + 8320 - 4, e01); sts32(cb + 8320, e23);
                    sts64(cb + 12480 - 8, u2{om0, o12});
                }
            }
        }
        bar();
        STAMP(3);
        auto emit = [&](int x) {
            u2 y[4];
#pragma unroll
            for (int ch = 0; ch < 4; ch++) y[ch] = lds64(sb + YB + ch * 4096 + x * 8);
            char* op = (char*)P.out + (size_t)x * 4 * 1792 + d0 * 2;
#pragma unroll
            for (int i = 0; i < 4; i++) {
                const u32 sel = (i & 1) ? 0x07060302u : 0x05040100u;
                *(u2*)(op + i * 1792) = u2{__builtin_amdgcn_perm(y[1][i >> 1], y[0][i >> 1], sel), __builtin_amdgcn_perm(y[3][i >> 1], y[2][i >> 1], sel)};
            }
        };
        const int m = lane & 15, g = lane >> 4, sg = (m + 1) & 3;
        u32 ua = sb + UB + w * 16640 + sg * 4160 + 2 * (1 + m - sg + 8 * g), ub = ua + 8;
        asm volatile("" : "+v"(ub));
        const u32 ka = sb + KB + w * 4608 + 16 * (33 + 2 * m - g), ya = sb + YB + w * 4096 + 32 * m + 8 * g;
        if (w < 4) conv_phase<0, 5>(ua, ub, ka, ya);
        bar();
        STAMP(4);
        if (w < 4) conv_phase<5, 8>(ua, ub, ka, ya);
        else { emit(tid - 256); if (w == 4) emit(tid); }
        bar();
        STAMP(5);
        if (tid < 192) emit(320 + tid);
        STAMP(6);
    } else {
        const int j = idx - 14, quarter = xcd >> 1;
        const int ntile = quarter < 2 ? 6 : 5;
        if (tid == 0) __hip_atomic_store(flags + 448 + xcd * 18 + j, tag, __ATOMIC_RELAXED, __HIP_MEMORY_SCOPE_AGENT);
        const bool q6 = j >= 3 * ntile;
        if (q6 && xcd == 7 && j == 17) {
            if (w == 0) {
                for (int n = 0; n < SPIN_CAP; n++) {
                    bool ok = true;
#pragma unroll
                    for (int i = 0; i < 10; i++) {
                        const int f = lane + 64 * i;
                        if (f < 592) ok &= __hip_atomic_load(flags + f, __ATOMIC_RELAXED, __HIP_MEMORY_SCOPE_AGENT) == tag;
                    }
                    if (__all(ok)) break;
                    __builtin_amdgcn_s_sleep(8);
                }
                if (lane == 0) __hip_atomic_store((GAS u64*)P.ws, tag, __ATOMIC_RELAXED, __HIP_MEMORY_SCOPE_AGENT);
            }
            return;
        }
        int group, tile;
        if (!q6) { group = 3 * (xcd & 1) + j % 3; tile = (quarter < 2 ? 6 * quarter : 5 * quarter + 2) + j / 3; }
        else { group = 6; tile = 2 * ((xcd - 4) * 3 + j - 15); }
        const int win = group == 0 ? 0 : group == 1 ? 160 : group == 2 ? 320 : group == 3 ? 448 : group == 4 ? 576 : 704;
        const int head0 = group <= 3 ? 3 * group : group == 4 ? 11 : 13;
        const int nh = q6 ? 0 : (group == 3 || group == 4) ? 2 : 3;
        const bool has2 = j < 16;
        const int T2 = 4 * xcd + (j >> 2), ct2 = j & 3;
        const int th = w & 1, rw = w >> 1;
        const bool isq = rw == 3, act = isq || rw < nh;
        u32 wv, xv, ov; bool wact, oact;
        {
            const int r = 2 * w + (lane >> 5), f = (r & 3) | ((r >> 3) & 1) << 2, su = (lane & 31) ^ (f << 1);
            const int bc = su < 12 ? 896 + win + 16 * su : su < 24 ? 1792 + win + 16 * (su - 12) : group * 128 + 16 * (su - 24);
            wv = r * 2688 + bc; wact = !q6 || su >= 24;
            const int xr = 8 * w + (lane >> 3);
            xv = xr * 896 + 16 * ((lane & 7) ^ ((xr >> 1) & 7));
            const int ou = (lane & 15) ^ ((lane >> 4) << 2);
            ov = (4 * w + (lane >> 4)) * 896 + ct2 * 256 + 16 * ou; oact = ct2 < 3 || ou < 8;
        }
        const int l15 = lane & 15, g4 = lane >> 4;
        u32 xa = 32768 + (48 * th + l15) * 128 + 16 * (g4 ^ (l15 >> 1));
        u32 wa[4];
        {
            const int q = l15 >> 2, p = lane & 3, f = q | (g4 & 1) << 2;
            const int o = (head0 + rw) * 56 - win;
#pragma unroll
            for (int ct = 0; ct < 4; ct++) {
                const int bc = isq ? 384 + 64 * (ct >> 1) + 16 * p + 8 * ((ct & 1) ^ (p >> 1)) : o + 192 * (ct >> 1) + 32 * (ct & 1) + 8 * p;
                wa[ct] = (8 * g4 + q) * 512 + (bc ^ (f << 5));
            }
        }
        const int nround = q6 ? 2 : 1;
        for (int round = 0; round < nround; round++, tile++) {
            const int row0 = tile < 21 ? 96 * tile : 1952;
            const char* xb = (const char*)P.x + (size_t)row0 * 896;
            const bool last = round == nround - 1;
            auto issue = [&](int c, u32 slot) {
                const char* wb = (const char*)P.wqkv + (size_t)c * 64 * 2688;
                if (wact) {
#pragma unroll
                    for (int k = 0; k < 4; k++) dma16(wb + k * 16 * 2688, wv, slot + (w + 8 * k) * 1024);
                }
                dma16(xb + c * 128, xv, slot + 32768 + w * 1024);
                if (w < 4) dma16(xb + c * 128 + 64 * 896, xv, slot + 32768 + (8 + w) * 1024);
            };
            auto issue_wo = [&](int i0, int n) {
                if (oact) for (int k = 0; k < n; k++) dma16((const char*)P.wo + (size_t)(i0 + 8 * k) * 4 * 896, ov, sb + 45056 + (i0 + 8 * k + w) * 1024);
            };
            if (round) bar();
            issue(0, sb); issue(1, sb + 45056); issue(2, sb + 90112);
            f4 acc[3][4];
#pragma unroll
            for (int tt = 0; tt < 3; tt++)
#pragma unroll
                for (int ct = 0; ct < 4; ct++) { const float v = (ct < 2 || isq) ? 1.f : 0.f; acc[tt][ct] = f4{v, v, v, v}; }
            u32 slot = sb;
            const bool wo_now = last && has2;
#pragma unroll 1
            for (int c = 0; c < 7; c++) {
                if (c == 0) { if (w < 4) WAITV(12); else WAITV(10); }
                else if (c < 6) { if (w < 4) WAITV(6); else WAITV(5); }
                else {
                    if (wo_now) WAITV(8); else WAITV(0);
                    u64 fl = tag;
                    if (lane < 56) fl = *(LAS u64*)(sb + 159744 + w * 448 + lane * 8);
                    for (int n = 0; n < SPIN_CAP && !__all(fl == tag); n++) {
                        __builtin_amdgcn_s_sleep(2);
                        if (lane < 56) fl = __hip_atomic_load(flags + w * 56 + lane, __ATOMIC_RELAXED, __HIP_MEMORY_SCOPE_AGENT);
                    }
                }
                bar();
                if (c == 0) STAMP(1);
                if (c == 6) STAMP(2);
                const u32 prev = slot == sb ? sb + 90112 : slot - 45056;
                if (c == 4 && lane < 28) dma16c(P.ws + WS_FLAG + w * 448, lane * 16, sb + 159744 + w * 448);
                if (c >= 1 && c <= 4) issue(c + 2, prev);
                if (wo_now) { if (c == 5) { issue_wo(0, 5); issue_wo(88, 3); } if (c == 6) issue_wo(40, 6); }
                if (act) {
#pragma unroll
                    for (int s = 0; s < 2; s++) {
                        u4 xf[3], wf[4];
#pragma unroll
                        for (int tt = 0; tt < 3; tt++) xf[tt] = lds128(slot + (xa ^ (64 * s)) + 2048 * tt);
#pragma unroll
                        for (int ct = 0; ct < 4; ct++) {
                            u2 lo = ldstr(slot + wa[ct] + 16384 * s), hi = ldstr(slot + wa[ct] + 16384 * s + 2048);
                            wf[ct] = u4{lo.x, lo.y, hi.x, hi.y};
                        }
                        if (isq) {
#pragma unroll
                            for (int ct = 0; ct < 4; ct++)
#pragma unroll
                                for (int tt = 0; tt < 3; tt++) acc[tt][ct] = mfma16(wf[ct], xf[tt], acc[tt][ct]);
                        } else {
#pragma unroll
                            for (int ct = 0; ct < 4; ct++)
#pragma unroll
                                for (int tt = 0; tt < 3; tt++) acc[tt][ct] = mfma16(xf[tt], wf[ct], acc[tt][ct]);
                        }
                    }
                }
                slot = slot == sb + 90112 ? sb : slot + 45056;
            }
            STAMP(3);
            const int tok0 = row0 + 48 * th;
            if (isq) {
#pragma unroll
                for (int tt = 0; tt < 3; tt++) {
                    const int tk = tok0 + 16 * tt;
                    if (tk >= 96 * tile) {
#pragma unroll
                        for (int pr = 0; pr < 2; pr++) {
                            const f4 a = acc[tt][2 * pr], c = acc[tt][2 * pr + 1];
                            const u4 v = {san(pk(phi1(a[0]), phi1(a[1]))), san(pk(phi1(a[2]), phi1(a[3]))), san(pk(phi1(c[0]), phi1(c[1]))), san(pk(phi1(c[2]), phi1(c[3])))};
                            __builtin_amdgcn_raw_buffer_store_b128(v, rex, EX_PQ + (((tk >> 5) * 56 + 8 * group + 4 * pr + g4) * 32 + (tk & 31) + l15) * 16, 0, 16);
                        }
                    }
                }
            } else if (act) {
                f4 kv[2][2];
#pragma unroll
                for (int i = 0; i < 4; i++) kv[i >> 1][i & 1] = f4{0.f, 0.f, 0.f, 0.f};
#pragma unroll
                for (int tt = 0; tt < 3; tt++) {
                    if (tok0 + 16 * tt >= 96 * tile) {
                        u2 pf[2], vf[2];
#pragma unroll
                        for (int i = 0; i < 2; i++) {
                            const f4 a = acc[tt][i], c = acc[tt][2 + i];
                            pf[i] = u2{pk(phi1(a[0]), phi1(a[1])), pk(phi1(a[2]), phi1(a[3]))};
                            vf[i] = u2{pk(c[0], c[1]), pk(c[2], c[3])};
                        }
                        if (l15 == 12) vf[1] = u2{0x3F803F80u, 0x3F803F80u};
#pragma unroll
                        for (int i = 0; i < 4; i++) kv[i >> 1][i & 1] = mfma16k16(pf[i >> 1], vf[i & 1], kv[i >> 1][i & 1]);
                    }
                }
#pragma unroll
                for (int i = 0; i < 4; i++) {
                    const int d4 = 4 * (i >> 1) + g4, e = 16 * (i & 1) + l15;
                    const u4 r = __builtin_bit_cast(u4, kv[i >> 1][i & 1]);
                    const u32 off = (d4 < 7 && e <= 28) ? EX_PART + ((((head0 + rw) * 7 + d4) * 44 + 2 * tile + th) * 32 + e) * 16 : 0x80000000u;
                    __builtin_amdgcn_raw_buffer_store_b128(u4{san(r.x), san(r.y), san(r.z), san(r.w)}, rex, off, 0, 16);
                }
            }
            STAMP(4);
        }
        if (!has2) goto done;
        if (act && !isq) WAITV(4); else WAITV(0);
        bar();
        if (w == 0 && j < 14) {
            const int c4 = xcd * 14 + j, e = lane & 31;
            f4 sum;
            for (int n = 0; n < SPIN_CAP; n++) {
                u4 v[22];
                asm volatile("" ::: "memory");
#pragma unroll
                for (int i = 0; i < 22; i++) v[i] = __builtin_amdgcn_raw_buffer_load_b128(rex, EX_PART + c4 * 22528 + lane * 16 + i * 1024, 0, 16);
                u32 mx = 0;
                sum = f4{0.f, 0.f, 0.f, 0.f};
#pragma unroll
                for (int i = 0; i < 22; i++) { mx = max4u(mx, v[i]); sum += __builtin_bit_cast(f4, v[i]); }
                if (__all(mx != SENT || e > 28)) break;
                __builtin_amdgcn_s_sleep(1);
            }
#pragma unroll
            for (int i = 0; i < 4; i++) sum[i] += __shfl_xor(sum[i], 32);
            const int Pp = c4 / 14, cl = c4 % 14, U = c4 >> 1, ul = U - 7 * Pp, hp = cl >= 7;
            const int half = (c4 & 1) ^ ((U >> 1) & 1);
            const int s = hp ? (ul >= 5) : (ul >> 1), hh = hp ? (ul == 3 || ul == 6) : (ul & 1);
            u2 v = {san(pk(sum[0], sum[1])), san(pk(sum[2], sum[3]))};
            if (e > 28 || lane >= 32) v = u2{0u, 0u};
            const u32 off = (lane < 32 || ul == 3) ? EX_KVF + ((2 * Pp + hp) * 2 + s) * 1024 + hh * 512 + e * 16 + 8 * (half ^ (lane >> 5)) : 0x80000000u;
            __builtin_amdgcn_raw_buffer_store_b64(v, rex, off, 0, 16);
        }
        STAMP(5);
        {
            const int hh = lane >> 5, l31 = lane & 31, tg2 = w & 1, cgp = w >> 1;
            const bool act2 = ct2 < 3 || cgp < 2;
            u4 wof[28];
            {
                const int q = l15 >> 2, p = lane & 3;
                const u32 a0 = sb + 45056 + (8 * hh + q) * 256 + ((64 * cgp + 32 * (p & 1) + 16 * (g4 & 1) + 8 * (p >> 1)) ^ (q << 6));
#pragma unroll
                for (int ks = 0; ks < 28; ks++) {
                    u2 lo = ldstr(a0 + 4096 * ks), hi = ldstr(a0 + 4096 * ks + 1024);
                    wof[ks] = u4{lo.x, lo.y, hi.x, hi.y};
                }
            }
            u4 Rq[2][4], F[4];
            for (int n = 0; n < SPIN_CAP; n++) {
                asm volatile("" ::: "memory");
                u32 mx = 0;
#pragma unroll
                for (int tg = 0; tg < 2; tg++)
#pragma unroll
                    for (int i = 0; i < 4; i++) {
                        const int ul = i == 0 ? hh : i == 1 ? 2 + hh : i == 2 ? 4 - hh : 5 + hh;
                        Rq[tg][i] = __builtin_amdgcn_raw_buffer_load_b128(rex, EX_PQ + (((2 * T2 + tg) * 56 + 7 * w + ul) * 32 + l31) * 16, 0, 16);
                    }
#pragma unroll
                for (int i = 0; i < 8; i++) mx = max4u(mx, Rq[i >> 2][i & 3]);
                if (__all(mx != SENT)) break;
                __builtin_amdgcn_s_sleep(1);
            }
            STAMP(6);
            bar();
            for (int n = 0; n < SPIN_CAP; n++) {
                asm volatile("" ::: "memory");
                u32 mx = 0;
#pragma unroll
                for (int i = 0; i < 4; i++) F[i] = __builtin_amdgcn_raw_buffer_load_b128(rex, EX_KVF + (4 * w + i) * 1024 + lane * 16, 0, 16);
#pragma unroll
                for (int i = 0; i < 4; i++) mx = max4u(mx, F[i]);
                if (__all(mx != SENT)) break;
                __builtin_amdgcn_s_sleep(1);
            }
            STAMP(7);
#pragma unroll
            for (int hp = 0; hp < 2; hp++)
#pragma unroll
                for (int tg = 0; tg < 2; tg++) {
                    f16v nm;
#pragma unroll
                    for (int i = 0; i < 16; i++) nm[i] = 0.f;
                    nm = mfma32(F[2 * hp], Rq[tg][2 * hp], nm);
                    nm = mfma32(F[2 * hp + 1], Rq[tg][2 * hp + 1], nm);
                    const u32 dz = __float_as_uint(nm[12]);
                    const float den = __uint_as_float(__builtin_amdgcn_permlane32_swap(dz, dz, false, false)[1]);
                    const float rd = __builtin_amdgcn_rcpf(den + 1e-6f);
                    const u32 oa = sb + (32 * tg + l31) * 912 + 56 * (2 * w + hp) + 8 * hh;
#pragma unroll
                    for (int i = 0; i < 4; i++)
                        if (i < 3 || hh == 0) sts64(oa + 16 * i, u2{pk(nm[4 * i] * rd, nm[4 * i + 1] * rd), pk(nm[4 * i + 2] * rd, nm[4 * i + 3] * rd)});
                }
            bar();
            STAMP(8);
            if (act2) {
                f16v o;
#pragma unroll
                for (int i = 0; i < 16; i++) o[i] = 0.f;
                const u32 oa = sb + (32 * tg2 + l31) * 912 + 16 * hh;
#pragma unroll
                for (int ks = 0; ks < 28; ks++) o = mfma32(wof[ks], lds128(oa + 32 * ks), o);
                char* op = (char*)P.out + (size_t)(64 * T2 + 32 * tg2 + l31) * 1792 + 896 + (128 * ct2 + 32 * cgp + 16 * hh) * 2;
                *(u4*)op = u4{pk(o[0], o[1]), pk(o[2], o[3]), pk(o[4], o[5]), pk(o[6], o[7])};
                *(u4*)(op + 16) = u4{pk(o[8], o[9]), pk(o[10], o[11]), pk(o[12], o[13]), pk(o[14], o[15])};
            }
            STAMP(9);
        }
    }
done:;
}

extern "C" void kernel_launch(void* const* d_in, const int* in_sizes, int n_in, void* d_out, int out_size, void* d_ws, size_t ws_size,
                              hipStream_t stream) {
    Params P;
    P.u = (const u16*)d_in[0]; P.Are = (const u16*)d_in[1]; P.Aim = (const u16*)d_in[2]; P.Bre = (const u16*)d_in[3];
    P.Bim = (const u16*)d_in[4]; P.Cre = (const u16*)d_in[5]; P.Cim = (const u16*)d_in[6]; P.ldt = (const u16*)d_in[7];
    P.Dsk = (const u16*)d_in[8]; P.x = (const u16*)d_in[9]; P.wqkv = (const u16*)d_in[10]; P.wo = (const u16*)d_in[11];
    P.out = (u16*)d_out; P.ws = (char*)d_ws;
    fused_kernel<<<256, 512, 0, stream>>>(P);
}
```

```cpp
#include <hip/hip_runtime.h>
#include <stdint.h>

typedef unsigned int u32;
typedef unsigned long long u64;
typedef __attribute__((ext_vector_type(16))) u32 u16v;

struct Srd { const void* p; u64 n; };
struct Params {
    Srd x, wqkv, wo, ws, out, u;
    const void *ldt, *Dsk, *Are, *Aim, *Bre, *Bim, *Cre, *Cim;
    u64 slot, zero;
};

__global__ __launch_bounds__(512, 2) void fused_kernel(Params P) {
    __shared__ __attribute__((aligned(16))) char smem[163840];
    const int tid = threadIdx.x, b = blockIdx.x ^ 3;
    u16v k0, k2;
    u32 stv, trv;
    asm volatile(
    "s_load_dwordx16 s[36:51], %[ka], 0x0\n\t"
    "s_load_dwordx16 s[52:67], %[ka], 0x40\n\t"
    "s_load_dwordx8 s[68:75], %[ka], 0x80\n\t"
    "v_readfirstlane_b32 s8, %[tid]\n\t"
    "s_and_b32 s9, %[bid], 7\n\t"
    "s_lshr_b32 s10, %[bid], 3\n\t"
    "s_getpc_b64 s[20:21]\n\t"
    ".Lpc0_%=:\n\t"
    "s_lshr_b32 s8, s8, 6\n\t"
    "v_and_b32 v1, 63, %[tid]\n\t"
    "s_lshl3_add_u32 s22, s10, s8\n\t"
    "v_bfrev_b32 v9, 1\n\t"
    "s_min_u32 s22, s22, (.LD7_%=-.Lpc0_%=-4)/128\n\t"
    "s_mov_b32 s35, 0\n\t"
    "v_lshlrev_b32_e64 v2, 7, s22\n\t"
    "global_load_dword v253, v2, s[20:21]\n\t"
    "s_cmp_lt_u32 s10, 16\n\t"
    "s_cbranch_scc0 .LA1_%=\n\t"
    "s_lshr_b32 s20, s9, 1\n\t"
    "s_lshr_b32 s21, s10, 2\n\t"
    "s_lshl2_add_u32 s11, s20, s21\n\t"
    "s_and_b32 s20, s9, 1\n\t"
    "s_and_b32 s21, s10, 3\n\t"
    "s_lshl2_add_u32 s12, s20, s21\n\t"
    "v_lshrrev_b32 v10, 2, v1\n\t"
    "v_and_b32 v7, 3, v1\n\t"
    "s_cmp_lt_u32 s8, 2\n\t"
    "s_cbranch_scc1 .LR3_%=n\n\t"
    "s_mov_b32 s24, 1058679466\n\t"
    "s_mov_b32 s25, 0\n\t"
    "v_lshlrev_b32 v5, 3, v1\n\t"
    "v_cndmask_b32 v34, 1, 0, s[24:25]\n\t"
    "s_mov_b32 s24, 1056964608\n\t"
    "v_add_u32 v5, 160832, v5\n\t"
    "v_cndmask_b32 v35, 1, 0, s[24:25]\n\t"
    "v_mov_b32 v8, 1\n\t"
    "ds_write2st64_b64 v5, v[34:35], v[8:9] offset1:1\n\t"
    "s_waitcnt lgkmcnt(0)\n\t"
    "s_barrier\n\t"
    "s_branch .LR3_%=\n\t"
    ".LR3_%=n:\n\t"
    "v_lshrrev_b32 v11, 4, v1\n\t"
    "v_lshrrev_b32 v12, 3, v1\n\t"
    "v_xor_b32 v11, v11, v1\n\t"
    "v_mul_u32_u24 v12, 0x380, v12\n\t"
    "v_and_b32 v11, 7, v11\n\t"
    "v_mul_u32_u24 v13, 69, v1\n\t"
    "s_mul_i32 s14, s11, 114688\n\t"
    "v_lshl_add_u32 v12, v11, 4, v12\n\t"
    "v_lshrrev_b32 v13, 10, v13\n\t"
    "s_mul_i32 s27, s12, 112\n\t"
    "v_mad_i32_i24 v14, v13, -15, v1\n\t"
    "v_mul_u32_u24 v13, 0x2a00, v13\n\t"
    "v_add_u32 v12, s14, v12\n\t"
    "v_cmp_eq_u32 vcc, 7, v14\n\t"
    "v_lshl_add_u32 v11, v14, 4, v13\n\t"
    "v_lshrrev_b32 v13, 3, v14\n\t"
    "s_add_u32 s34, s27, 896\n\t"
    "v_cndmask_b32 v11, v11, v9, vcc\n\t"
    "v_mul_u32_u24 v14, 0x300, v13\n\t"
    "s_cmp_eq_u32 s8, 0\n\t"
    "s_cbranch_scc0 .Llw8_%=\n\t"
    "v_xor_b32 v5, 64, v12\n\t"
    "v_mov_b32 v4, v12\n\t"
    "v_add_u32 v2, 12288, v12\n\t"
    "v_add_u32 v3, 18432, v5\n\t"
    "v_add_u32 v5, 6144, v5\n\t"
    "s_mov_b32 m0, 98304\n\t"
    "s_mov_b32 s14, 0\n\t"
    "s_waitcnt lgkmcnt(0)\n\t"
    "buffer_load_dwordx4 v4, s[36:39], s14 offen offset:0 lds\n\t"
    "buffer_load_dwordx4 v5, s[36:39], s14 offen offset:1024 lds\n\t"
    "buffer_load_dwordx4 v2, s[36:39], s14 offen offset:2048 lds\n\t"
    "buffer_load_dwordx4 v3, s[36:39], s14 offen offset:3072 lds\n\t"
    "s_add_u32 m0, m0, 4096\n\t"
    "s_add_u32 s14, s14, 28672\n\t"
    "buffer_load_dwordx4 v4, s[36:39], s14 offen offset:0 lds\n\t"
    "buffer_load_dwordx4 v5, s[36:39], s14 offen offset:1024 lds\n\t"
    "buffer_load_dwordx4 v2, s[36:39], s14 offen offset:2048 lds\n\t"
    "buffer_load_dwordx4 v3, s[36:39], s14 offen offset:3072 lds\n\t"
    "s_add_u32 m0, m0, 4096\n\t"
    "s_add_u32 s14, s14, 28672\n\t"
    "buffer_load_dwordx4 v4, s[36:39], s14 offen offset:0 lds\n\t"
    "buffer_load_dwordx4 v5, s[36:39], s14 offen offset:1024 lds\n\t"
    "buffer_load_dwordx4 v2, s[36:39], s14 offen offset:2048 lds\n\t"
    "buffer_load_dwordx4 v3, s[36:39], s14 offen offset:3072 lds\n\t"
    "s_add_u32 m0, m0, 4096\n\t"
    "s_add_u32 s14, s14, 28672\n\t"
    "buffer_load_dwordx4 v4, s[36:39], s14 offen offset:0 lds\n\t"
    "buffer_load_dwordx4 v5, s[36:39], s14 offen offset:1024 lds\n\t"
    "buffer_load_dwordx4 v2, s[36:39], s14 offen offset:2048 lds\n\t"
    "buffer_load_dwordx4 v3, s[36:39], s14 offen offset:3072 lds\n\t"
    "s_add_u32 m0, m0, 4096\n\t"
    "s_add_u32 s14, s14, 28672\n\t"
    "v_lshlrev_b32 v6, 2, v7\n\t"
    "s_mov_b32 s25, 160832\n\t"
    "s_mov_b32 s15, 0\n\t"
    "v_add_u32 v6, 161008, v6\n\t"
    "v_mov_b32 v8, 0\n\t"
    "s_barrier\n\t"
    ".Llx9_%=:\n\t"
    "s_mov_b32 m0, s25\n\t"
    "s_mov_b64 exec, 1\n\t"
    "buffer_load_dword v9, s[36:39], 0 offen lds\n\t"
    "s_mov_b64 exec, -1\n\t"
    "s_add_u32 s14, s14, -114560\n\t"
    "s_waitcnt lgkmcnt(0)\n\t"
    "v_cmp_ne_u32 vcc, 0, v8\n\t"
    "s_mov_b32 m0, s15\n\t"
    "s_cbranch_vccnz .Lfs11_%=\n\t"
    ".Lfo12_%=:\n\t"
    "buffer_load_dwordx4 v4, s[36:39], s14 offen offset:0 lds\n\t"
    "buffer_load_dwordx4 v5, s[36:39], s14 offen offset:1024 lds\n\t"
    "buffer_load_dwordx4 v2, s[36:39], s14 offen offset:2048 lds\n\t"
    "buffer_load_dwordx4 v3, s[36:39], s14 offen offset:3072 lds\n\t"
    "s_add_u32 m0, m0, 4096\n\t"
    "s_add_u32 s14, s14, 28672\n\t"
    "buffer_load_dwordx4 v4, s[36:39], s14 offen offset:0 lds\n\t"
    "buffer_load_dwordx4 v5, s[36:39], s14 offen offset:1024 lds\n\t"
    "buffer_load_dwordx4 v2, s[36:39], s14 offen offset:2048 lds\n\t"
    "buffer_load_dwordx4 v3, s[36:39], s14 offen offset:3072 lds\n\t"
    "s_add_u32 m0, m0, 4096\n\t"
    "s_add_u32 s14, s14, 28672\n\t"
    "v_add_u32 v6, 16, v6\n\t"
    "ds_read_b32 v8, v6\n\t"
    "buffer_load_dwordx4 v4, s[36:39], s14 offen offset:0 lds\n\t"
    "buffer_load_dwordx4 v5, s[36:39], s14 offen offset:1024 lds\n\t"
    "buffer_load_dwordx4 v2, s[36:39], s14 offen offset:2048 lds\n\t"
    "buffer_load_dwordx4 v3, s[36:39], s14 offen offset:3072 lds\n\t"
    "s_add_u32 m0, m0, 4096\n\t"
    "s_add_u32 s14, s14, 28672\n\t"
    "buffer_load_dwordx4 v4, s[36:39], s14 offen offset:0 lds\n\t"
    "buffer_load_dwordx4 v5, s[36:39], s14 offen offset:1024 lds\n\t"
    "buffer_load_dwordx4 v2, s[36:39], s14 offen offset:2048 lds\n\t"
    "buffer_load_dwordx4 v3, s[36:39], s14 offen offset:3072 lds\n\t"
    "s_add_u32 m0, m0, 4096\n\t"
    "s_add_u32 s14, s14, 28672\n\t"
    "s_add_u32 s25, s25, 16\n\t"
    "s_mov_b32 s15, m0\n\t"
    "s_cmp_lg_u32 s25, 160928\n\t"
    "s_cbranch_scc1 .Llx9_%=\n\t"
    "s_mov_b32 m0, s25\n\t"
    "s_mov_b64 exec, 1\n\t"
    "buffer_load_dword v9, s[36:39], 0 offen lds\n\t"
    "s_mov_b64 exec, -1\n\t"
    "s_branch .Lwo10_%=\n\t"
    ".Lfs11_%=:\n\t"
    ".Lp13_%=:\n\t"
    "ds_read_b32 v8, v6\n\t"
    "s_add_u32 s35, s35, 1\n\t"
    "s_cmp_lt_u32 s35, 0x400000\n\t"
    "s_waitcnt lgkmcnt(0)\n\t"
    "v_cmp_ne_u32 vcc, 0, v8\n\t"
    "s_cbranch_vccz .Lo14_%=\n\t"
    "s_sleep 1\n\t"
    "s_cbranch_scc1 .Lp13_%=\n\t"
    ".Lo14_%=:\n\t"
    "s_branch .Lfo12_%=\n\t"
    ".Llw8_%=:\n\t"
    "v_add3_u32 v4, v14, v11, s34\n\t"
    "s_mov_b32 m0, 66560\n\t"
    "s_setprio 1\n\t"
    "s_mov_b32 s28, -1\n\t"
    "v_add_u32 v5, 1728, v4\n\t"
    "v_add_u32 v2, 3456, v4\n\t"
    "v_add_u32 v3, 5184, v4\n\t"
    "s_mov_b32 s29, 0xfffffff\n\t"
    "s_mov_b32 s14, 0\n\t"
    "s_mov_b64 exec, s[28:29]\n\t"
    "s_waitcnt lgkmcnt(0)\n\t"
    "buffer_load_dwordx4 v4, s[40:43], s14 offen offset:0 lds\n\t"
    "buffer_load_dwordx4 v5, s[40:43], s14 offen offset:960 lds\n\t"
    "buffer_load_dwordx4 v2, s[40:43], s14 offen offset:1920 lds\n\t"
    "buffer_load_dwordx4 v3, s[40:43], s14 offen offset:2880 lds\n\t"
    "s_add_u32 m0, m0, 3840\n\t"
    "s_add_u32 s14, s14, 43008\n\t"
    "buffer_load_dwordx4 v4, s[40:43], s14 offen offset:0 lds\n\t"
    "buffer_load_dwordx4 v5, s[40:43], s14 offen offset:960 lds\n\t"
    "buffer_load_dwordx4 v2, s[40:43], s14 offen offset:1920 lds\n\t"
    "buffer_load_dwordx4 v3, s[40:43], s14 offen offset:2880 lds\n\t"
    "s_add_u32 m0, m0, 3840\n\t"
    "s_add_u32 s14, s14, 43008\n\t"
    "v_lshlrev_b32 v6, 2, v7\n\t"
    "s_mov_b32 s25, 160836\n\t"
    "v_mul_u32_u24 v13, 85120, v13\n\t"
    "v_add_u32 v6, 161008, v6\n\t"
    "v_subrev_u32 v13, 896, v13\n\t"
    "s_barrier\n\t"
    ".Llw15_%=:\n\t"
    "s_mov_b32 s15, m0\n\t"
    "s_mov_b32 m0, s25\n\t"
    "s_mov_b64 exec, 1\n\t"
    "buffer_load_dword v9, s[36:39], 0 offen lds\n\t"
    "s_mov_b64 exec, s[28:29]\n\t"
    "v_add_u32 v6, 16, v6\n\t"
    "s_mov_b32 m0, s15\n\t"
    "ds_read_b32 v8, v6\n\t"
    "buffer_load_dwordx4 v4, s[40:43], s14 offen offset:0 lds\n\t"
    "buffer_load_dwordx4 v5, s[40:43], s14 offen offset:960 lds\n\t"
    "buffer_load_dwordx4 v2, s[40:43], s14 offen offset:1920 lds\n\t"
    "buffer_load_dwordx4 v3, s[40:43], s14 offen offset:2880 lds\n\t"
    "s_add_u32 m0, m0, 3840\n\t"
    "s_add_u32 s14, s14, 43008\n\t"
    "buffer_load_dwordx4 v4, s[40:43], s14 offen offset:0 lds\n\t"
    "buffer_load_dwordx4 v5, s[40:43], s14 offen offset:960 lds\n\t"
    "buffer_load_dwordx4 v2, s[40:43], s14 offen offset:1920 lds\n\t"
    "buffer_load_dwordx4 v3, s[40:43], s14 offen offset:2880 lds\n\t"
    "s_add_u32 m0, m0, 3840\n\t"
    "s_add_u32 s14, s14, 43008\n\t"
    "s_mov_b32 s15, m0\n\t"
    "s_add_u32 m0, s25, 8\n\t"
    "s_mov_b64 exec, 1\n\t"
    "buffer_load_dword v9, s[36:39], 0 offen lds\n\t"
    "s_mov_b64 exec, s[28:29]\n\t"
    "s_add_u32 s25, s25, 16\n\t"
    "s_cmp_eq_u32 s25, 160948\n\t"
    "s_cbranch_scc1 .Llq16_%=\n\t"
    "s_cmp_eq_u32 s15, 160768\n\t"
    "s_cselect_b32 m0, 99328, s15\n\t"
    "s_cmp_eq_u32 s15, 97280\n\t"
    "s_waitcnt lgkmcnt(0)\n\t"
    "v_cmp_ne_u32 vcc, 0, v8\n\t"
    "s_cselect_b32 m0, 114688, m0\n\t"
    "s_cbranch_vccnz .Lfs17_%=\n\t"
    ".Lfo18_%=:\n\t"
    "buffer_load_dwordx4 v4, s[40:43], s14 offen offset:0 lds\n\t"
    "buffer_load_dwordx4 v5, s[40:43], s14 offen offset:960 lds\n\t"
    "buffer_load_dwordx4 v2, s[40:43], s14 offen offset:1920 lds\n\t"
    "buffer_load_dwordx4 v3, s[40:43], s14 offen offset:2880 lds\n\t"
    "s_add_u32 m0, m0, 3840\n\t"
    "s_add_u32 s14, s14, 43008\n\t"
    "buffer_load_dwordx4 v4, s[40:43], s14 offen offset:0 lds\n\t"
    "buffer_load_dwordx4 v5, s[40:43], s14 offen offset:960 lds\n\t"
    "buffer_load_dwordx4 v2, s[40:43], s14 offen offset:1920 lds\n\t"
    "buffer_load_dwordx4 v3, s[40:43], s14 offen offset:2880 lds\n\t"
    "s_add_u32 m0, m0, 3840\n\t"
    "s_add_u32 s14, s14, 43008\n\t"
    "s_branch .Llw15_%=\n\t"
    ".Lfs17_%=:\n\t"
    ".Lp19_%=:\n\t"
    "ds_read_b32 v8, v6\n\t"
    "s_add_u32 s35, s35, 1\n\t"
    "s_cmp_lt_u32 s35, 0x400000\n\t"
    "s_waitcnt lgkmcnt(0)\n\t"
    "v_cmp_ne_u32 vcc, 0, v8\n\t"
    "s_cbranch_vccz .Lo20_%=\n\t"
    "s_sleep 1\n\t"
    "s_cbranch_scc1 .Lp19_%=\n\t"
    ".Lo20_%=:\n\t"
    "s_branch .Lfo18_%=\n\t"
    ".Llq16_%=:\n\t"
    "v_add_u32 v4, v4, v13\n\t"
    "v_add_u32 v5, v5, v13\n\t"
    "v_add_u32 v2, v2, v13\n\t"
    "v_add_u32 v3, v3, v13\n\t"
    "s_mov_b64 exec, -1\n\t"
    "v_mov_b32 v12, 160952\n\t"
    "v_lshlrev_b32 v16, 4, v1\n\t"
    "s_lshl4_add_u32 s22, s12, s11\n\t"
    "s_lshl_b32 s22, s22, 7\n\t"
    "s_add_u32 s22, s22, 18432\n\t"
    ".Lp21_%=:\n\t"
    "ds_read_b32 v8, v12\n\t"
    "s_add_u32 s35, s35, 1\n\t"
    "s_cmp_lt_u32 s35, 0x400000\n\t"
    "s_waitcnt lgkmcnt(0)\n\t"
    "v_cmp_ne_u32 vcc, 0, v8\n\t"
    "s_cbranch_vccz .Lo22_%=\n\t"
    "s_sleep 1\n\t"
    "s_cbranch_scc1 .Lp21_%=\n\t"
    ".Lo22_%=:\n\t"
    "ds_read_b64 v[18:19], v12 offset:456\n\t"
    "s_mov_b64 exec, s[28:29]\n\t"
    "s_add_u32 s14, s14, 86016\n\t"
    ".Llq16_%=x:\n\t"
    ".Lp23_%=:\n\t"
    "ds_read_b32 v8, v6\n\t"
    "s_add_u32 s35, s35, 1\n\t"
    "s_cmp_lt_u32 s35, 0x400000\n\t"
    "s_waitcnt lgkmcnt(0)\n\t"
    "v_cmp_ne_u32 vcc, 0, v8\n\t"
    "s_cbranch_vccz .Lo24_%=\n\t"
    "s_sleep 1\n\t"
    "s_cbranch_scc1 .Lp23_%=\n\t"
    ".Lo24_%=:\n\t"
    "s_memrealtime s[80:81]\n\t"
    "s_cmp_eq_u32 s25, 160964\n\t"
    "s_cbranch_scc0 .Llq16_%=z\n\t"
    "s_memtime s[60:61]\n\t"
    "s_memrealtime s[62:63]\n\t"
    ".Llq16_%=z:\n\t"
    "s_add_u32 s14, s14, 86016\n\t"
    "s_sub_u32 s34, s14, 1204224\n\t"
    "s_min_u32 s14, s14, s34\n\t"
    "s_cmp_eq_u32 s15, 160768\n\t"
    "s_cselect_b32 m0, 99328, s15\n\t"
    "s_nop 0\n\t"
    "buffer_load_dwordx4 v4, s[40:43], s14 offen offset:0 lds\n\t"
    "buffer_load_dwordx4 v5, s[40:43], s14 offen offset:960 lds\n\t"
    "buffer_load_dwordx4 v2, s[40:43], s14 offen offset:1920 lds\n\t"
    "buffer_load_dwordx4 v3, s[40:43], s14 offen offset:2880 lds\n\t"
    "s_add_u32 m0, m0, 3840\n\t"
    "s_add_u32 s14, s14, 43008\n\t"
    "buffer_load_dwordx4 v4, s[40:43], s14 offen offset:0 lds\n\t"
    "buffer_load_dwordx4 v5, s[40:43], s14 offen offset:960 lds\n\t"
    "buffer_load_dwordx4 v2, s[40:43], s14 offen offset:1920 lds\n\t"
    "buffer_load_dwordx4 v3, s[40:43], s14 offen offset:2880 lds\n\t"
    "s_add_u32 m0, m0, 3840\n\t"
    "s_add_u32 s14, s14, 43008\n\t"
    "s_cmp_eq_u32 s25, 160996\n\t"
    "s_cbranch_scc1 .Llq16_%=y\n\t"
    "s_add_u32 s14, s14, 86016\n\t"
    "s_sub_u32 s34, s14, 1204224\n\t"
    "s_min_u32 s14, s14, s34\n\t"
    "buffer_load_dwordx4 v4, s[40:43], s14 offen offset:0 lds\n\t"
    "buffer_load_dwordx4 v5, s[40:43], s14 offen offset:960 lds\n\t"
    "buffer_load_dwordx4 v2, s[40:43], s14 offen offset:1920 lds\n\t"
    "buffer_load_dwordx4 v3, s[40:43], s14 offen offset:2880 lds\n\t"
    "s_add_u32 m0, m0, 3840\n\t"
    "s_add_u32 s14, s14, 43008\n\t"
    "buffer_load_dwordx4 v4, s[40:43], s14 offen offset:0 lds\n\t"
    "buffer_load_dwordx4 v5, s[40:43], s14 offen offset:960 lds\n\t"
    "buffer_load_dwordx4 v2, s[40:43], s14 offen offset:1920 lds\n\t"
    "buffer_load_dwordx4 v3, s[40:43], s14 offen offset:2880 lds\n\t"
    "s_add_u32 m0, m0, 3840\n\t"
    "s_add_u32 s14, s14, 43008\n\t"
    "s_mov_b32 s15, m0\n\t"
    "s_add_u32 m0, s25, 8\n\t"
    "s_mov_b64 exec, 1\n\t"
    "buffer_load_dword v9, s[36:39], 0 offen lds\n\t"
    "s_mov_b64 exec, s[28:29]\n\t"
    "v_add_u32 v6, 16, v6\n\t"
    "s_add_u32 s25, s25, 16\n\t"
    "s_branch .Llq16_%=x\n\t"
    ".Llq16_%=y:\n\t"
    "s_mov_b32 m0, s25\n\t"
    "s_mov_b64 exec, 1\n\t"
    "buffer_load_dword v9, s[36:39], 0 offen lds\n\t"
    "s_mov_b64 exec, -1\n\t"
    "s_setprio 0\n\t"
    "v_lshlrev_b32 v6, 2, v7\n\t"
    "v_add_u32 v6, 161072, v6\n\t"
    "s_branch .Lwo10_%=\n\t"
    ".LR3_%=:\n\t"
    "s_cmp_lt_u32 s8, 4\n\t"
    "s_cbranch_scc0 .LC2_%=\n\t"
    "v_lshlrev_b32 v6, 2, v7\n\t"
    "v_add_u32 v6, 161072, v6\n\t"
    "s_cmp_eq_u32 s8, 2\n\t"
    "s_waitcnt lgkmcnt(0)\n\t"
    "s_load_dwordx2 s[78:79], s[48:49], 0x0\n\t"
    "s_cbranch_scc0 .Lr25_%=\n\t"
    "v_mov_b32 v16, 0\n\t"
    "v_mov_b32 v12, 160952\n\t"
    "s_waitcnt lgkmcnt(0)\n\t"
    "s_add_u32 s78, s78, 0x7f4a7c15\n\t"
    "s_addc_u32 s79, s79, 0x9e3779b9\n\t"
    "v_mov_b64 v[18:19], s[78:79]\n\t"
    "ds_write_b64 v12, v[18:19] offset:456\n\t"
    "ds_write_b32 v12, v16\n\t"
    "v_lshlrev_b32 v17, 4, v1\n\t"
    "s_lshl4_add_u32 s22, s12, s11\n\t"
    "s_lshl_b32 s22, s22, 7\n\t"
    "s_add_u32 s22, s22, 18432\n\t"
    ".Lr25_%=:\n\t"
    ".Lp26_%=:\n\t"
    "ds_read_b32 v8, v6 offset:32\n\t"
    "s_add_u32 s35, s35, 1\n\t"
    "s_cmp_lt_u32 s35, 0x400000\n\t"
    "s_waitcnt lgkmcnt(0)\n\t"
    "v_cmp_ne_u32 vcc, 0, v8\n\t"
    "s_cbranch_vccz .Lo27_%=\n\t"
    "s_sleep 2\n\t"
    "s_cbranch_scc1 .Lp26_%=\n\t"
    ".Lo27_%=:\n\t"
    ".Lwo10_%=:\n\t"
    "s_and_b32 s27, s12, 1\n\t"
    "s_bfe_u32 s26, s12, 0x10001\n\t"
    "v_lshrrev_b32 v11, 4, v1\n\t"
    "v_and_b32 v12, 15, v1\n\t"
    "s_mul_i32 s26, s26, 448\n\t"
    "s_mul_i32 s25, s27, 192\n\t"
    "s_add_u32 s30, s8, 1\n\t"
    "s_lshr_b32 s30, s30, 1\n\t"
    "v_and_b32 v13, 1, v11\n\t"
    "v_lshrrev_b32 v14, 1, v11\n\t"
    "s_add_u32 s26, s26, s25\n\t"
    "s_mul_i32 s17, s30, 3584\n\t"
    "s_and_b32 s31, s30, 1\n\t"
    "v_lshl_add_u32 v13, v13, 2, v14\n\t"
    "s_mul_i32 s31, s31, 1792\n\t"
    "v_mul_u32_u24 v11, 0xe0, v11\n\t"
    "s_add_u32 s17, s17, s26\n\t"
    "v_mul_u32_u24 v13, 0x380, v13\n\t"
    "s_sub_u32 s16, s17, s31\n\t"
    "s_add_u32 s17, s17, s31\n\t"
    "s_mul_i32 s25, s30, 896\n\t"
    "s_lshl_b32 s26, s27, 5\n\t"
    "v_lshl_add_u32 v24, v12, 4, v13\n\t"
    "s_sub_u32 s25, s25, s26\n\t"
    "v_lshl_add_u32 v5, v12, 4, v11\n\t"
    "s_lshl_b32 s27, s27, 1\n\t"
    "v_lshlrev_b32 v6, 2, v7\n\t"
    "s_lshl_b32 s28, 0x3fff3fff, s27\n\t"
    "v_add_u32 v5, s25, v5\n\t"
    "s_mov_b32 s29, s28\n\t"
    "v_add_u32 v6, 161072, v6\n\t"
    "v_add_u32 v16, 122880, v5\n\t"
    "v_mul_u32_u24 v11, 74, v1\n\t"
    "s_add_u32 s19, s25, s26\n\t"
    "v_lshrrev_b32 v11, 10, v11\n\t"
    "s_mov_b32 s20, -1\n\t"
    "s_movk_i32 s18, 0x380\n\t"
    "v_mad_i32_i24 v12, v11, -14, v1\n\t"
    "v_and_b32 v13, 1, v11\n\t"
    "v_lshrrev_b32 v11, 1, v11\n\t"
    "v_lshl_add_u32 v12, v12, 4, s26\n\t"
    "v_lshl_add_u32 v13, v13, 2, v11\n\t"
    "s_mov_b32 s21, 0xffffff\n\t"
    "v_mad_u32_u24 v28, v13, s18, v12\n\t"
    "s_sub_u32 s18, s8, s30\n\t"
    "s_and_b32 s19, s8, 1\n\t"
    "s_mul_i32 s34, s18, 896\n\t"
    "s_mul_i32 s18, s18, 3584\n\t"
    "s_mul_i32 s19, s19, 1792\n\t"
    "v_add_u32 v25, s34, v5\n\t"
    "s_add_u32 s18, s18, s16\n\t"
    "v_add_u32 v27, s34, v16\n\t"
    "s_add_u32 s18, s18, s31\n\t"
    "s_sub_u32 s18, s18, s19\n\t"
    "s_cmp_eq_u32 s8, 1\n\t"
    "s_cbranch_scc1 .Lwo10_%=a\n\t"
    "s_mov_b64 exec, s[28:29]\n\t"
    "s_add_u32 s14, s16, 0\n\t"
    "buffer_load_dwordx4 v[96:99], v24, s[44:47], s14 offen\n\t"
    "s_add_u32 s14, s17, 8960\n\t"
    "buffer_load_dwordx4 v[100:103], v24, s[44:47], s14 offen\n\t"
    "s_add_u32 s14, s16, 21504\n\t"
    "buffer_load_dwordx4 v[104:107], v24, s[44:47], s14 offen\n\t"
    "s_add_u32 s14, s17, 30464\n\t"
    "buffer_load_dwordx4 v[108:111], v24, s[44:47], s14 offen\n\t"
    "s_cmp_eq_u32 s8, 2\n\t"
    "s_cbranch_scc0 .Lr25_%=h\n\t"
    ".Lp28_%=:\n\t"
    "ds_read_b32 v8, v6 offset:48\n\t"
    "s_add_u32 s35, s35, 1\n\t"
    "s_cmp_lt_u32 s35, 0x400000\n\t"
    "s_waitcnt lgkmcnt(0)\n\t"
    "v_cmp_ne_u32 vcc, 0, v8\n\t"
    "s_cbranch_vccz .Lo29_%=\n\t"
    "s_sleep 1\n\t"
    "s_cbranch_scc1 .Lp28_%=\n\t"
    ".Lo29_%=:\n\t"
    "s_memrealtime s[80:81]\n\t"
    "s_mov_b64 exec, 255\n\t"
    "s_waitcnt lgkmcnt(0)\n\t"
    "v_mov_b32 v20, s80\n\t"
    "buffer_store_dwordx4 v[18:21], v17, s[48:51], s22 offen sc1\n\t"
    "s_mov_b64 exec, s[28:29]\n\t"
    ".Lr25_%=h:\n\t"
    "s_add_u32 s14, s16, 43008\n\t"
    "s_waitcnt vmcnt(3)\n\t"
    "buffer_load_dwordx4 v[112:115], v24, s[44:47], s14 offen\n\t"
    "s_add_u32 s14, s17, 51968\n\t"
    "s_waitcnt vmcnt(3)\n\t"
    "buffer_load_dwordx4 v[116:119], v24, s[44:47], s14 offen\n\t"
    "s_add_u32 s14, s16, 64512\n\t"
    "s_waitcnt vmcnt(3)\n\t"
    "buffer_load_dwordx4 v[120:123], v24, s[44:47], s14 offen\n\t"
    "s_add_u32 s14, s17, 73472\n\t"
    "s_waitcnt vmcnt(3)\n\t"
    "buffer_load_dwordx4 v[124:127], v24, s[44:47], s14 offen\n\t"
    "s_add_u32 s14, s16, 86016\n\t"
    "s_waitcnt vmcnt(3)\n\t"
    "buffer_load_dwordx4 v[128:131], v24, s[44:47], s14 offen\n\t"
    "s_add_u32 s14, s17, 94976\n\t"
    "s_waitcnt vmcnt(3)\n\t"
    "buffer_load_dwordx4 v[132:135], v24, s[44:47], s14 offen\n\t"
    "s_add_u32 s14, s16, 107520\n\t"
    "s_waitcnt vmcnt(3)\n\t"
    "buffer_load_dwordx4 v[136:139], v24, s[44:47], s14 offen\n\t"
    "s_add_u32 s14, s17, 116480\n\t"
    "s_waitcnt vmcnt(3)\n\t"
    "buffer_load_dwordx4 v[140:143], v24, s[44:47], s14 offen\n\t"
    "s_add_u32 s14, s16, 129024\n\t"
    "buffer_load_dwordx4 v[144:147], v24, s[44:47], s14 offen\n\t"
    "s_add_u32 s14, s17, 137984\n\t"
    "buffer_load_dwordx4 v[148:151], v24, s[44:47], s14 offen\n\t"
    "s_add_u32 s14, s16, 150528\n\t"
    "buffer_load_dwordx4 v[152:155], v24, s[44:47], s14 offen\n\t"
    "s_add_u32 s14, s17, 159488\n\t"
    "buffer_load_dwordx4 v[156:159], v24, s[44:47], s14 offen\n\t"
    "s_mov_b64 exec, -1\n\t"
    ".Lwo10_%=a:\n\t"
    "v_mov_b32 v12, 160952\n\t"
    "v_and_b32 v3, 31, v1\n\t"
    "v_lshrrev_b32 v2, 5, v1\n\t"
    "v_lshlrev_b32 v10, 4, v1\n\t"
    "v_mul_u32_u24 v13, 0x390, v3\n\t"
    "s_mul_i32 s25, s8, 56\n\t"
    "v_lshl_add_u32 v13, v2, 3, v13\n\t"
    "s_add_u32 s25, s25, 64512\n\t"
    "v_add_u32 v13, s25, v13\n\t"
    "s_lshr_b32 s31, s12, 2\n\t"
    "s_xor_b32 s30, s11, 4\n\t"
    "s_lshl1_add_u32 s31, s30, s31\n\t"
    "s_lshl_b32 s31, s31, 5\n\t"
    "s_add_u32 s31, s31, s8\n\t"
    "s_lshl_b32 s31, s31, 11\n\t"
    "s_add_u32 s31, s31, 1232896\n\t"
    "s_lshl_b32 s30, s8, 11\n\t"
    "s_add_u32 s30, s30, 53248\n\t"
    "s_add_u32 s23, s30, 98304\n\t"
    "s_lshl2_add_u32 s25, s8, 161000\n\t"
    "v_mov_b32 v15, s25\n\t"
    "v_mov_b32 v4, 0\n\t"
    "s_mov_b32 s24, 0x358637bd\n\t"
    "s_mov_b32 s27, 0xffff\n\t"
    "s_movk_i32 s26, 300\n\t"
    ".Lp30_%=:\n\t"
    "ds_read_b32 v8, v12\n\t"
    "s_add_u32 s35, s35, 1\n\t"
    "s_cmp_lt_u32 s35, 0x400000\n\t"
    "s_waitcnt lgkmcnt(0)\n\t"
    "v_cmp_ne_u32 vcc, 0, v8\n\t"
    "s_cbranch_vccz .Lo31_%=\n\t"
    "s_sleep 2\n\t"
    "s_cbranch_scc1 .Lp30_%=\n\t"
    ".Lo31_%=:\n\t"
    "ds_read_b64 v[18:19], v12 offset:456\n\t"
    "s_waitcnt lgkmcnt(0)\n\t"
    "v_readfirstlane_b32 s78, v18\n\t"
    "v_readfirstlane_b32 s79, v19\n\t"
    "v_lshrrev_b64 v[18:19], v10, v[18:19]\n\t"
    "v_and_b32 v2, 48, v1\n\t"
    "v_lshlrev_b32 v18, 16, v18\n\t"
    "v_add_u32 v2, v2, v6\n\t"
    ".Lp32_%=:\n\t"
    "ds_read_b32 v8, v6 offset:128\n\t"
    "s_add_u32 s35, s35, 1\n\t"
    "s_cmp_lt_u32 s35, 0x400000\n\t"
    "s_waitcnt lgkmcnt(0)\n\t"
    "v_cmp_ne_u32 vcc, 0, v8\n\t"
    "s_cbranch_vccz .Lo33_%=\n\t"
    "s_sleep 1\n\t"
    "s_cbranch_scc1 .Lp32_%=\n\t"
    ".Lo33_%=:\n\t"
    ".Lp34_%=:\n\t"
    "ds_read_b32 v8, v6 offset:96\n\t"
    "s_add_u32 s35, s35, 1\n\t"
    "s_cmp_lt_u32 s35, 0x400000\n\t"
    "s_waitcnt lgkmcnt(0)\n\t"
    "v_cmp_ne_u32 vcc, 0, v8\n\t"
    "s_cbranch_vccz .Lo35_%=\n\t"
    "s_sleep 1\n\t"
    "s_cbranch_scc1 .Lp34_%=\n\t"
    ".Lo35_%=:\n\t"
    "s_mul_i32 s19, s8, 896\n\t"
    "s_mov_b64 exec, s[20:21]\n\t"
    "s_add_u32 m0, s19, 122880\n\t"
    "s_add_u32 s14, s18, 258048\n\t"
    "buffer_load_dwordx4 v28, s[44:47], s14 offen lds\n\t"
    "s_add_u32 m0, s19, 126464\n\t"
    "s_add_u32 s14, s18, 272384\n\t"
    "buffer_load_dwordx4 v28, s[44:47], s14 offen lds\n\t"
    "s_add_u32 m0, s19, 130048\n\t"
    "s_add_u32 s14, s18, 286720\n\t"
    "buffer_load_dwordx4 v28, s[44:47], s14 offen lds\n\t"
    "s_add_u32 m0, s19, 133632\n\t"
    "s_add_u32 s14, s18, 301056\n\t"
    "buffer_load_dwordx4 v28, s[44:47], s14 offen lds\n\t"
    "s_add_u32 m0, s19, 137216\n\t"
    "s_add_u32 s14, s18, 315392\n\t"
    "buffer_load_dwordx4 v28, s[44:47], s14 offen lds\n\t"
    "s_add_u32 m0, s19, 140800\n\t"
    "s_add_u32 s14, s18, 329728\n\t"
    "buffer_load_dwordx4 v28, s[44:47], s14 offen lds\n\t"
    "s_mov_b64 exec, s[28:29]\n\t"
    "s_add_u32 s14, s18, 215040\n\t"
    "buffer_load_dwordx4 v[160:163], v24, s[44:47], s14 offen\n\t"
    "s_add_u32 s14, s18, 229376\n\t"
    "buffer_load_dwordx4 v[164:167], v24, s[44:47], s14 offen\n\t"
    "s_add_u32 s14, s18, 243712\n\t"
    "buffer_load_dwordx4 v[168:171], v24, s[44:47], s14 offen\n\t"
    "s_add_u32 s14, s18, 344064\n\t"
    "buffer_load_dwordx4 v[172:175], v24, s[44:47], s14 offen\n\t"
    "s_add_u32 s14, s18, 358400\n\t"
    "buffer_load_dwordx4 v[176:179], v24, s[44:47], s14 offen\n\t"
    "s_add_u32 s14, s18, 372736\n\t"
    "buffer_load_dwordx4 v[180:183], v24, s[44:47], s14 offen\n\t"
    "s_add_u32 s14, s18, 387072\n\t"
    "buffer_load_dwordx4 v[184:187], v24, s[44:47], s14 offen\n\t"
    "s_bitcmp1_b32 s8, 0\n\t"
    "s_cbranch_scc0 .Lwo10_%=f\n\t"
    "s_add_u32 s14, s18, 172032\n\t"
    "buffer_load_dwordx4 v[188:191], v24, s[44:47], s14 offen\n\t"
    "s_add_u32 s14, s18, 170240\n\t"
    "buffer_load_dwordx4 v[192:195], v24, s[44:47], s14 offen\n\t"
    "s_add_u32 s14, s18, 186368\n\t"
    "buffer_load_dwordx4 v[196:199], v24, s[44:47], s14 offen\n\t"
    "s_add_u32 s14, s18, 184576\n\t"
    "buffer_load_dwordx4 v[200:203], v24, s[44:47], s14 offen\n\t"
    "s_add_u32 s14, s18, 200704\n\t"
    "buffer_load_dwordx4 v[204:207], v24, s[44:47], s14 offen\n\t"
    "s_add_u32 s14, s18, 198912\n\t"
    "buffer_load_dwordx4 v[208:211], v24, s[44:47], s14 offen\n\t"
    ".Lwo10_%=f:\n\t"
    "s_mov_b64 exec, -1\n\t"
    "s_cmp_eq_u32 s8, 1\n\t"
    "s_cbranch_scc0 .Lwo10_%=c\n\t"
    "s_mov_b32 s34, 8\n\t"
    ".Lgl36_%=:\n\t"
    "s_memrealtime s[80:81]\n\t"
    "s_waitcnt lgkmcnt(0)\n\t"
    "s_bfe_u32 s83, s80, 196613\n\t"
    "s_lshl_b32 s83, s83, 7\n\t"
    "s_sub_u32 s34, s34, 1\n\t"
    "s_cbranch_scc1 .Lgl36_%=v\n\t"
    "s_buffer_load_dwordx4 s[80:83], s[48:51], s83 offset:128\n\t"
    "s_waitcnt lgkmcnt(0)\n\t"
    "s_branch .Lgs38_%=\n\t"
    ".Lgl36_%=v:\n\t"
    "buffer_load_dwordx3 v[20:22], v4, s[48:51], s83 offen offset:128 sc1\n\t"
    "s_mov_b32 s34, 0\n\t"
    "s_waitcnt vmcnt(0)\n\t"
    "v_readfirstlane_b32 s80, v20\n\t"
    "v_readfirstlane_b32 s81, v21\n\t"
    "v_readfirstlane_b32 s82, v22\n\t"
    ".Lgs38_%=:\n\t"
    "s_cmp_eq_u64 s[80:81], s[78:79]\n\t"
    "s_cbranch_scc1 .Lgo37_%=\n\t"
    "s_sleep 2\n\t"
    "s_add_u32 s35, s35, 16\n\t"
    "s_cmp_lt_u32 s35, 0x400000\n\t"
    "s_cbranch_scc1 .Lgl36_%=\n\t"
    ".Lgo37_%=:\n\t"
    "v_mov_b32 v5, s82\n\t"
    "s_sub_u32 s82, s82, s62\n\t"
    "s_mul_i32 s82, s82, 18\n\t"
    "s_add_u32 s82, s82, s60\n\t"
    "v_mov_b32 v20, s82\n\t"
    "ds_write_b32 v12, v20 offset:488\n\t"
    "ds_write_b64 v12, v[4:5] offset:464\n\t"
    ".Lwo10_%=c:\n\t"
    "s_mov_b64 exec, s[28:29]\n\t"
    ".Lp39_%=:\n\t"
    "ds_read_b32 v8, v6 offset:96\n\t"
    "s_add_u32 s35, s35, 1\n\t"
    "s_cmp_lt_u32 s35, 0x400000\n\t"
    "s_waitcnt lgkmcnt(0)\n\t"
    "v_cmp_ne_u32 vcc, 0, v8\n\t"
    "s_cbranch_vccz .Lo40_%=\n\t"
    "s_sleep 1\n\t"
    "s_cbranch_scc1 .Lp39_%=\n\t"
    ".Lo40_%=:\n\t"
    ".Lp41_%=:\n\t"
    "ds_read_b32 v8, v6 offset:160\n\t"
    "s_add_u32 s35, s35, 1\n\t"
    "s_cmp_lt_u32 s35, 0x400000\n\t"
    "s_waitcnt lgkmcnt(0)\n\t"
    "v_cmp_ne_u32 vcc, 0, v8\n\t"
    "s_cbranch_vccz .Lo42_%=\n\t"
    "s_sleep 1\n\t"
    "s_cbranch_scc1 .Lp41_%=\n\t"
    ".Lo42_%=:\n\t"
    "s_cmp_eq_u32 s8, 1\n\t"
    "s_cbranch_scc1 .Lwo10_%=e\n\t"
    "s_waitcnt vmcnt(7)\n\t"
    "ds_write_b128 v5, v[96:99] offset:0\n\t"
    "ds_write_b128 v5, v[100:103] offset:2688\n\t"
    "ds_write_b128 v5, v[104:107] offset:5376\n\t"
    "ds_write_b128 v5, v[108:111] offset:8064\n\t"
    "ds_write_b128 v5, v[112:115] offset:10752\n\t"
    "ds_write_b128 v5, v[116:119] offset:13440\n\t"
    "ds_write_b128 v5, v[120:123] offset:16128\n\t"
    "ds_write_b128 v5, v[124:127] offset:18816\n\t"
    "ds_write_b128 v5, v[128:131] offset:21504\n\t"
    "ds_write_b128 v5, v[132:135] offset:24192\n\t"
    "ds_write_b128 v5, v[136:139] offset:26880\n\t"
    "ds_write_b128 v5, v[140:143] offset:29568\n\t"
    "ds_write_b128 v5, v[144:147] offset:32256\n\t"
    "ds_write_b128 v5, v[148:151] offset:34944\n\t"
    "ds_write_b128 v5, v[152:155] offset:37632\n\t"
    "ds_write_b128 v5, v[156:159] offset:40320\n\t"
    ".Lwo10_%=e:\n\t"
    "s_waitcnt vmcnt(3)\n\t"
    "ds_write_b128 v25, v[160:163] offset:53760\n\t"
    "s_waitcnt vmcnt(0)\n\t"
    "s_bitcmp1_b32 s8, 0\n\t"
    "s_cbranch_scc0 .Lwo10_%=g\n\t"
    "ds_write_b128 v25, v[188:191] offset:43008\n\t"
    "ds_write_b128 v25, v[192:195] offset:42112\n\t"
    "ds_write_b128 v25, v[196:199] offset:46592\n\t"
    "ds_write_b128 v25, v[200:203] offset:45696\n\t"
    "ds_write_b128 v25, v[204:207] offset:50176\n\t"
    "ds_write_b128 v25, v[208:211] offset:49280\n\t"
    ".Lwo10_%=g:\n\t"
    ".Lwo10_%=b:\n\t"
    "s_mov_b64 exec, -1\n\t"
    "ds_write_b32 v15, v4 offset:424\n\t"
    "s_movk_i32 s34, 5200\n\t"
    "s_cmp_eq_u32 s9, 7\n\t"
    "s_cselect_b32 s34, 5720, s34\n\t"
    "s_branch .Ltp43_%=a\n\t"
    ".Ltp43_%=:\n\t"
    "s_sleep 1\n\t"
    "s_add_u32 s35, s35, 1\n\t"
    "s_cmp_lt_u32 s35, 0x400000\n\t"
    "s_cbranch_scc0 .Ltp43_%=c\n\t"
    ".Ltp43_%=a:\n\t"
    "ds_read_b64 v[20:21], v12 offset:464\n\t"
    "ds_read_b32 v22, v12 offset:488\n\t"
    ".Ltp43_%=b:\n\t"
    "s_memtime s[80:81]\n\t"
    "s_memrealtime s[82:83]\n\t"
    "s_waitcnt lgkmcnt(0)\n\t"
    "v_cmp_ne_u32 vcc, 0, v20\n\t"
    "v_sub_u32 v23, s80, v22\n\t"
    "v_sub_u32 v3, s82, v21\n\t"
    "s_cbranch_vccnz .Ltp43_%=\n\t"
    "v_cmp_gt_u32 vcc, s26, v3\n\t"
    "v_readfirstlane_b32 s80, v23\n\t"
    "v_readfirstlane_b32 s82, v22\n\t"
    "s_cbranch_vccnz .Ltp43_%=b\n\t"
    "s_sub_u32 s80, s34, s80\n\t"
    "s_cbranch_scc1 .Ltp43_%=c\n\t"
    "s_mul_i32 s80, s80, 1\n\t"
    "s_min_u32 s80, s80, 400\n\t"
    "s_add_u32 s34, s34, s80\n\t"
    ".Lu44_%=:\n\t"
    "s_memtime s[80:81]\n\t"
    "s_waitcnt lgkmcnt(0)\n\t"
    "s_sub_u32 s80, s80, s82\n\t"
    "s_cmp_lt_u32 s80, s34\n\t"
    "s_cbranch_scc1 .Lu44_%=\n\t"
    ".Ltp43_%=c:\n\t"
    "s_setprio 3\n\t"
    "s_add_u32 s25, s30, 0\n\t"
    "buffer_load_dwordx4 v[132:135], v10, s[48:51], s25 offen offset:1024 sc1\n\t"
    "buffer_load_dwordx4 v[128:131], v10, s[48:51], s25 offen offset:0 sc1\n\t"
    "s_add_u32 s25, s31, 0\n\t"
    "buffer_load_dwordx4 v[68:71], v10, s[48:51], s25 offen offset:1024 sc1\n\t"
    "buffer_load_dwordx4 v[64:67], v10, s[48:51], s25 offen offset:0 sc1\n\t"
    "s_mov_b64 exec, s[28:29]\n\t"
    "ds_write_b128 v25, v[164:167] offset:57344\n\t"
    "ds_write_b128 v25, v[168:171] offset:60928\n\t"
    "s_mov_b64 exec, -1\n\t"
    "s_setprio 2\n\t"
    "s_add_u32 s25, s31, 32768\n\t"
    "buffer_load_dwordx4 v[100:103], v10, s[48:51], s25 offen offset:1024 sc1\n\t"
    "buffer_load_dwordx4 v[96:99], v10, s[48:51], s25 offen offset:0 sc1\n\t"
    "s_mov_b64 exec, s[28:29]\n\t"
    "ds_write_b128 v27, v[172:175] offset:21504\n\t"
    "s_mov_b64 exec, -1\n\t"
    "s_add_u32 s25, s30, 8192\n\t"
    "buffer_load_dwordx4 v[140:143], v10, s[48:51], s25 offen offset:1024 sc1\n\t"
    "buffer_load_dwordx4 v[136:139], v10, s[48:51], s25 offen offset:0 sc1\n\t"
    "s_mov_b64 exec, s[28:29]\n\t"
    "ds_write_b128 v27, v[176:179] offset:25088\n\t"
    "s_mov_b64 exec, -1\n\t"
    "s_setprio 1\n\t"
    "s_add_u32 s25, s31, 8192\n\t"
    "buffer_load_dwordx4 v[76:79], v10, s[48:51], s25 offen offset:1024 sc1\n\t"
    "buffer_load_dwordx4 v[72:75], v10, s[48:51], s25 offen offset:0 sc1\n\t"
    "s_mov_b64 exec, s[28:29]\n\t"
    "ds_write_b128 v27, v[180:183] offset:28672\n\t"
    "s_mov_b64 exec, -1\n\t"
    "s_add_u32 s25, s31, 40960\n\t"
    "buffer_load_dwordx4 v[108:111], v10, s[48:51], s25 offen offset:1024 sc1\n\t"
    "buffer_load_dwordx4 v[104:107], v10, s[48:51], s25 offen offset:0 sc1\n\t"
    "s_mov_b64 exec, s[28:29]\n\t"
    "ds_write_b128 v27, v[184:187] offset:32256\n\t"
    "s_mov_b64 exec, -1\n\t"
    "ds_write_b32 v15, v4\n\t"
    "s_setprio 3\n\t"
    ".Lp45_%=:\n\t"
    "ds_read_b32 v8, v6 offset:176\n\t"
    "s_add_u32 s35, s35, 1\n\t"
    "s_cmp_lt_u32 s35, 0x400000\n\t"
    "s_waitcnt lgkmcnt(0)\n\t"
    "v_cmp_ne_u32 vcc, 0, v8\n\t"
    "s_cbranch_vccz .Lo46_%=\n\t"
    "s_sleep 1\n\t"
    "s_cbranch_scc1 .Lp45_%=\n\t"
    ".Lo46_%=:\n\t"
    "s_waitcnt vmcnt(8)\n\t"
    "v_xor_b32 v67, v67, v18\n\t"
    "v_xor_b32 v71, v71, v18\n\t"
    "v_xor_b32 v131, v131, v18\n\t"
    "v_xor_b32 v135, v135, v18\n\t"
    "v_or_b32 v26, v67, v71\n\t"
    "v_or3_b32 v26, v26, v131, v135\n\t"
    "v_cmp_lt_u32 vcc, s27, v26\n\t"
    "s_cbranch_vccnz .Lsl47_%=0\n\t"
    ".Lsl47_%=r0:\n\t"
    "v_mfma_f32_32x32x16_bf16 v[160:175], v[128:131], v[64:67], 0\n\t"
    "v_mfma_f32_32x32x16_bf16 v[160:175], v[132:135], v[68:71], v[160:175]\n\t"
    "s_add_u32 s25, s30, 16384\n\t"
    "buffer_load_dwordx4 v[148:151], v10, s[48:51], s25 offen offset:1024 sc1\n\t"
    "buffer_load_dwordx4 v[144:147], v10, s[48:51], s25 offen offset:0 sc1\n\t"
    "s_add_u32 s25, s31, 16384\n\t"
    "buffer_load_dwordx4 v[84:87], v10, s[48:51], s25 offen offset:1024 sc1\n\t"
    "buffer_load_dwordx4 v[80:83], v10, s[48:51], s25 offen offset:0 sc1\n\t"
    "s_nop 5\n\t"
    "v_add_f32 v216, s24, v172\n\t"
    "v_mov_b32 v217, v216\n\t"
    "s_nop 1\n\t"
    "v_permlane32_swap_b32 v217, v216\n\t"
    "v_rcp_f32 v216, v216\n\t"
    "s_nop 0\n\t"
    "v_mul_f32 v160, v160, v216\n\t"
    "v_mul_f32 v161, v161, v216\n\t"
    "v_mul_f32 v162, v162, v216\n\t"
    "v_mul_f32 v163, v163, v216\n\t"
    "v_mul_f32 v164, v164, v216\n\t"
    "v_mul_f32 v165, v165, v216\n\t"
    "v_mul_f32 v166, v166, v216\n\t"
    "v_mul_f32 v167, v167, v216\n\t"
    "v_cvt_pk_bf16_f32 v208, v160, v161\n\t"
    "v_cvt_pk_bf16_f32 v209, v162, v163\n\t"
    "ds_write_b64 v13, v[208:209] offset:0\n\t"
    "v_mul_f32 v168, v168, v216\n\t"
    "v_mul_f32 v169, v169, v216\n\t"
    "v_mul_f32 v170, v170, v216\n\t"
    "v_mul_f32 v171, v171, v216\n\t"
    "v_cvt_pk_bf16_f32 v210, v164, v165\n\t"
    "v_cvt_pk_bf16_f32 v211, v166, v167\n\t"
    "ds_write_b64 v13, v[210:211] offset:16\n\t"
    "v_mul_f32 v172, v172, v216\n\t"
    "v_mul_f32 v173, v173, v216\n\t"
    "v_mul_f32 v174, v174, v216\n\t"
    "v_mul_f32 v175, v175, v216\n\t"
    "v_cvt_pk_bf16_f32 v212, v168, v169\n\t"
    "v_cvt_pk_bf16_f32 v213, v170, v171\n\t"
    "ds_write_b64 v13, v[212:213] offset:32\n\t"
    "v_cvt_pk_bf16_f32 v214, v172, v173\n\t"
    "v_cvt_pk_bf16_f32 v215, v174, v175\n\t"
    "s_mov_b32 exec_hi, 0\n\t"
    "ds_write_b64 v13, v[214:215] offset:48\n\t"
    "ds_write_b32 v15, v4 offset:264\n\t"
    "s_mov_b32 exec_hi, -1\n\t"
    "s_waitcnt vmcnt(10)\n\t"
    "v_xor_b32 v99, v99, v18\n\t"
    "v_xor_b32 v103, v103, v18\n\t"
    "v_or_b32 v26, v99, v103\n\t"
    "v_cmp_lt_u32 vcc, s27, v26\n\t"
    "s_cbranch_vccnz .Lsl47_%=1\n\t"
    ".Lsl47_%=r1:\n\t"
    "v_mfma_f32_32x32x16_bf16 v[176:191], v[128:131], v[96:99], 0\n\t"
    "v_mfma_f32_32x32x16_bf16 v[176:191], v[132:135], v[100:103], v[176:191]\n\t"
    "s_add_u32 s25, s30, 24576\n\t"
    "buffer_load_dwordx4 v[156:159], v10, s[48:51], s25 offen offset:1024 sc1\n\t"
    "buffer_load_dwordx4 v[152:155], v10, s[48:51], s25 offen offset:0 sc1\n\t"
    "s_add_u32 s25, s31, 24576\n\t"
    "buffer_load_dwordx4 v[92:95], v10, s[48:51], s25 offen offset:1024 sc1\n\t"
    "buffer_load_dwordx4 v[88:91], v10, s[48:51], s25 offen offset:0 sc1\n\t"
    "s_waitcnt vmcnt(10)\n\t"
    "v_xor_b32 v75, v75, v18\n\t"
    "v_xor_b32 v79, v79, v18\n\t"
    "v_xor_b32 v139, v139, v18\n\t"
    "v_xor_b32 v143, v143, v18\n\t"
    "v_or_b32 v26, v75, v79\n\t"
    "v_or3_b32 v26, v26, v139, v143\n\t"
    "v_cmp_lt_u32 vcc, s27, v26\n\t"
    "s_cbranch_vccnz .Lsl47_%=2\n\t"
    ".Lsl47_%=r2:\n\t"
    "v_mfma_f32_32x32x16_bf16 v[192:207], v[136:139], v[72:75], 0\n\t"
    "v_mfma_f32_32x32x16_bf16 v[192:207], v[140:143], v[76:79], v[192:207]\n\t"
    "s_add_u32 s25, s31, 49152\n\t"
    "buffer_load_dwordx4 v[116:119], v10, s[48:51], s25 offen offset:1024 sc1\n\t"
    "buffer_load_dwordx4 v[112:115], v10, s[48:51], s25 offen offset:0 sc1\n\t"
    "s_add_u32 s25, s31, 57344\n\t"
    "buffer_load_dwordx4 v[124:127], v10, s[48:51], s25 offen offset:1024 sc1\n\t"
    "buffer_load_dwordx4 v[120:123], v10, s[48:51], s25 offen offset:0 sc1\n\t"
    "v_add_f32 v226, s24, v188\n\t"
    "v_mov_b32 v227, v226\n\t"
    "s_waitcnt vmcnt(12)\n\t"
    "v_xor_b32 v107, v107, v18\n\t"
    "v_xor_b32 v111, v111, v18\n\t"
    "v_permlane32_swap_b32 v227, v226\n\t"
    "v_rcp_f32 v226, v226\n\t"
    "v_or_b32 v26, v107, v111\n\t"
    "v_cmp_lt_u32 vcc, s27, v26\n\t"
    "v_mul_f32 v176, v176, v226\n\t"
    "v_mul_f32 v177, v177, v226\n\t"
    "v_mul_f32 v178, v178, v226\n\t"
    "v_mul_f32 v179, v179, v226\n\t"
    "s_cbranch_vccnz .Lsl47_%=3\n\t"
    ".Lsl47_%=r3:\n\t"
    "v_mfma_f32_32x32x16_bf16 v[160:175], v[136:139], v[104:107], 0\n\t"
    "v_mul_f32 v180, v180, v226\n\t"
    "v_mul_f32 v181, v181, v226\n\t"
    "v_mul_f32 v182, v182, v226\n\t"
    "v_mul_f32 v183, v183, v226\n\t"
    "v_cvt_pk_bf16_f32 v218, v176, v177\n\t"
    "v_cvt_pk_bf16_f32 v219, v178, v179\n\t"
    "ds_write_b64 v13, v[218:219] offset:29184\n\t"
    "v_mul_f32 v184, v184, v226\n\t"
    "v_mul_f32 v185, v185, v226\n\t"
    "v_mul_f32 v186, v186, v226\n\t"
    "v_mul_f32 v187, v187, v226\n\t"
    "v_cvt_pk_bf16_f32 v220, v180, v181\n\t"
    "v_cvt_pk_bf16_f32 v221, v182, v183\n\t"
    "ds_write_b64 v13, v[220:221] offset:29200\n\t"
    "v_mfma_f32_32x32x16_bf16 v[160:175], v[140:143], v[108:111], v[160:175]\n\t"
    "v_mul_f32 v188, v188, v226\n\t"
    "v_mul_f32 v189, v189, v226\n\t"
    "v_mul_f32 v190, v190, v226\n\t"
    "v_mul_f32 v191, v191, v226\n\t"
    "v_cvt_pk_bf16_f32 v222, v184, v185\n\t"
    "v_cvt_pk_bf16_f32 v223, v186, v187\n\t"
    "ds_write_b64 v13, v[222:223] offset:29216\n\t"
    "v_cvt_pk_bf16_f32 v224, v188, v189\n\t"
    "v_cvt_pk_bf16_f32 v225, v190, v191\n\t"
    "s_mov_b32 exec_hi, 0\n\t"
    "ds_write_b64 v13, v[224:225] offset:29232\n\t"
    "ds_write_b32 v15, v4 offset:280\n\t"
    "s_mov_b32 exec_hi, -1\n\t"
    "v_add_f32 v216, s24, v204\n\t"
    "v_mov_b32 v217, v216\n\t"
    "s_waitcnt vmcnt(8)\n\t"
    "v_xor_b32 v83, v83, v18\n\t"
    "v_xor_b32 v87, v87, v18\n\t"
    "v_permlane32_swap_b32 v217, v216\n\t"
    "v_rcp_f32 v216, v216\n\t"
    "v_xor_b32 v147, v147, v18\n\t"
    "v_xor_b32 v151, v151, v18\n\t"
    "v_or_b32 v26, v83, v87\n\t"
    "v_or3_b32 v26, v26, v147, v151\n\t"
    "v_cmp_lt_u32 vcc, s27, v26\n\t"
    "v_mul_f32 v192, v192, v216\n\t"
    "v_mul_f32 v193, v193, v216\n\t"
    "v_mul_f32 v194, v194, v216\n\t"
    "v_mul_f32 v195, v195, v216\n\t"
    "s_cbranch_vccnz .Lsl47_%=4\n\t"
    ".Lsl47_%=r4:\n\t"
    "v_mfma_f32_32x32x16_bf16 v[176:191], v[144:147], v[80:83], 0\n\t"
    "v_mul_f32 v196, v196, v216\n\t"
    "v_mul_f32 v197, v197, v216\n\t"
    "v_mul_f32 v198, v198, v216\n\t"
    "v_mul_f32 v199, v199, v216\n\t"
    "v_cvt_pk_bf16_f32 v208, v192, v193\n\t"
    "v_cvt_pk_bf16_f32 v209, v194, v195\n\t"
    "ds_write_b64 v13, v[208:209] offset:224\n\t"
    "v_mul_f32 v200, v200, v216\n\t"
    "v_mul_f32 v201, v201, v216\n\t"
    "v_mul_f32 v202, v202, v216\n\t"
    "v_mul_f32 v203, v203, v216\n\t"
    "v_cvt_pk_bf16_f32 v210, v196, v197\n\t"
    "v_cvt_pk_bf16_f32 v211, v198, v199\n\t"
    "ds_write_b64 v13, v[210:211] offset:240\n\t"
    "v_mfma_f32_32x32x16_bf16 v[176:191], v[148:151], v[84:87], v[176:191]\n\t"
    "v_mul_f32 v204, v204, v216\n\t"
    "v_mul_f32 v205, v205, v216\n\t"
    "v_mul_f32 v206, v206, v216\n\t"
    "v_mul_f32 v207, v207, v216\n\t"
    "v_cvt_pk_bf16_f32 v212, v200, v201\n\t"
    "v_cvt_pk_bf16_f32 v213, v202, v203\n\t"
    "ds_write_b64 v13, v[212:213] offset:256\n\t"
    "v_cvt_pk_bf16_f32 v214, v204, v205\n\t"
    "v_cvt_pk_bf16_f32 v215, v206, v207\n\t"
    "s_mov_b32 exec_hi, 0\n\t"
    "ds_write_b64 v13, v[214:215] offset:272\n\t"
    "ds_write_b32 v15, v4 offset:296\n\t"
    "s_mov_b32 exec_hi, -1\n\t"
    "v_add_f32 v226, s24, v172\n\t"
    "v_mov_b32 v227, v226\n\t"
    "s_waitcnt vmcnt(4)\n\t"
    "v_xor_b32 v91, v91, v18\n\t"
    "v_xor_b32 v95, v95, v18\n\t"
    "v_permlane32_swap_b32 v227, v226\n\t"
    "v_rcp_f32 v226, v226\n\t"
    "v_xor_b32 v155, v155, v18\n\t"
    "v_xor_b32 v159, v159, v18\n\t"
    "v_or_b32 v26, v91, v95\n\t"
    "v_or3_b32 v26, v26, v155, v159\n\t"
    "v_cmp_lt_u32 vcc, s27, v26\n\t"
    "v_mul_f32 v160, v160, v226\n\t"
    "v_mul_f32 v161, v161, v226\n\t"
    "v_mul_f32 v162, v162, v226\n\t"
    "v_mul_f32 v163, v163, v226\n\t"
    "s_cbranch_vccnz .Lsl47_%=5\n\t"
    ".Lsl47_%=r5:\n\t"
    "v_mfma_f32_32x32x16_bf16 v[192:207], v[152:155], v[88:91], 0\n\t"
    "v_mul_f32 v164, v164, v226\n\t"
    "v_mul_f32 v165, v165, v226\n\t"
    "v_mul_f32 v166, v166, v226\n\t"
    "v_mul_f32 v167, v167, v226\n\t"
    "v_cvt_pk_bf16_f32 v218, v160, v161\n\t"
    "v_cvt_pk_bf16_f32 v219, v162, v163\n\t"
    "ds_write_b64 v13, v[218:219] offset:29408\n\t"
    "v_mul_f32 v168, v168, v226\n\t"
    "v_mul_f32 v169, v169, v226\n\t"
    "v_mul_f32 v170, v170, v226\n\t"
    "v_mul_f32 v171, v171, v226\n\t"
    "v_cvt_pk_bf16_f32 v220, v164, v165\n\t"
    "v_cvt_pk_bf16_f32 v221, v166, v167\n\t"
    "ds_write_b64 v13, v[220:221] offset:29424\n\t"
    "v_mfma_f32_32x32x16_bf16 v[192:207], v[156:159], v[92:95], v[192:207]\n\t"
    "v_mul_f32 v172, v172, v226\n\t"
    "v_mul_f32 v173, v173, v226\n\t"
    "v_mul_f32 v174, v174, v226\n\t"
    "v_mul_f32 v175, v175, v226\n\t"
    "v_cvt_pk_bf16_f32 v222, v168, v169\n\t"
    "v_cvt_pk_bf16_f32 v223, v170, v171\n\t"
    "ds_write_b64 v13, v[222:223] offset:29440\n\t"
    "v_cvt_pk_bf16_f32 v224, v172, v173\n\t"
    "v_cvt_pk_bf16_f32 v225, v174, v175\n\t"
    "s_mov_b32 exec_hi, 0\n\t"
    "ds_write_b64 v13, v[224:225] offset:29456\n\t"
    "ds_write_b32 v15, v4 offset:312\n\t"
    "s_mov_b32 exec_hi, -1\n\t"
    "v_add_f32 v216, s24, v188\n\t"
    "v_mov_b32 v217, v216\n\t"
    "s_waitcnt vmcnt(2)\n\t"
    "v_xor_b32 v115, v115, v18\n\t"
    "v_xor_b32 v119, v119, v18\n\t"
    "v_permlane32_swap_b32 v217, v216\n\t"
    "v_rcp_f32 v216, v216\n\t"
    "v_or_b32 v26, v115, v119\n\t"
    "v_cmp_lt_u32 vcc, s27, v26\n\t"
    "v_mul_f32 v176, v176, v216\n\t"
    "v_mul_f32 v177, v177, v216\n\t"
    "v_mul_f32 v178, v178, v216\n\t"
    "v_mul_f32 v179, v179, v216\n\t"
    "s_cbranch_vccnz .Lsl47_%=6\n\t"
    ".Lsl47_%=r6:\n\t"
    "v_mfma_f32_32x32x16_bf16 v[160:175], v[144:147], v[112:115], 0\n\t"
    "v_mul_f32 v180, v180, v216\n\t"
    "v_mul_f32 v181, v181, v216\n\t"
    "v_mul_f32 v182, v182, v216\n\t"
    "v_mul_f32 v183, v183, v216\n\t"
    "v_cvt_pk_bf16_f32 v208, v176, v177\n\t"
    "v_cvt_pk_bf16_f32 v209, v178, v179\n\t"
    "ds_write_b64 v13, v[208:209] offset:448\n\t"
    "v_mul_f32 v184, v184, v216\n\t"
    "v_mul_f32 v185, v185, v216\n\t"
    "v_mul_f32 v186, v186, v216\n\t"
    "v_mul_f32 v187, v187, v216\n\t"
    "v_cvt_pk_bf16_f32 v210, v180, v181\n\t"
    "v_cvt_pk_bf16_f32 v211, v182, v183\n\t"
    "ds_write_b64 v13, v[210:211] offset:464\n\t"
    "v_mfma_f32_32x32x16_bf16 v[160:175], v[148:151], v[116:119], v[160:175]\n\t"
    "v_mul_f32 v188, v188, v216\n\t"
    "v_mul_f32 v189, v189, v216\n\t"
    "v_mul_f32 v190, v190, v216\n\t"
    "v_mul_f32 v191, v191, v216\n\t"
    "v_cvt_pk_bf16_f32 v212, v184, v185\n\t"
    "v_cvt_pk_bf16_f32 v213, v186, v187\n\t"
    "ds_write_b64 v13, v[212:213] offset:480\n\t"
    "v_cvt_pk_bf16_f32 v214, v188, v189\n\t"
    "v_cvt_pk_bf16_f32 v215, v190, v191\n\t"
    "s_mov_b32 exec_hi, 0\n\t"
    "ds_write_b64 v13, v[214:215] offset:496\n\t"
    "ds_write_b32 v15, v4 offset:328\n\t"
    "s_mov_b32 exec_hi, -1\n\t"
    "v_add_f32 v226, s24, v204\n\t"
    "v_mov_b32 v227, v226\n\t"
    "s_waitcnt vmcnt(0)\n\t"
    "v_xor_b32 v123, v123, v18\n\t"
    "v_xor_b32 v127, v127, v18\n\t"
    "v_permlane32_swap_b32 v227, v226\n\t"
    "v_rcp_f32 v226, v226\n\t"
    "v_or_b32 v26, v123, v127\n\t"
    "v_cmp_lt_u32 vcc, s27, v26\n\t"
    "v_mul_f32 v192, v192, v226\n\t"
    "v_mul_f32 v193, v193, v226\n\t"
    "v_mul_f32 v194, v194, v226\n\t"
    "v_mul_f32 v195, v195, v226\n\t"
    "s_cbranch_vccnz .Lsl47_%=7\n\t"
    ".Lsl47_%=r7:\n\t"
    "v_mfma_f32_32x32x16_bf16 v[176:191], v[152:155], v[120:123], 0\n\t"
    "v_mul_f32 v196, v196, v226\n\t"
    "v_mul_f32 v197, v197, v226\n\t"
    "v_mul_f32 v198, v198, v226\n\t"
    "v_mul_f32 v199, v199, v226\n\t"
    "v_cvt_pk_bf16_f32 v218, v192, v193\n\t"
    "v_cvt_pk_bf16_f32 v219, v194, v195\n\t"
    "ds_write_b64 v13, v[218:219] offset:672\n\t"
    "v_mul_f32 v200, v200, v226\n\t"
    "v_mul_f32 v201, v201, v226\n\t"
    "v_mul_f32 v202, v202, v226\n\t"
    "v_mul_f32 v203, v203, v226\n\t"
    "v_cvt_pk_bf16_f32 v220, v196, v197\n\t"
    "v_cvt_pk_bf16_f32 v221, v198, v199\n\t"
    "ds_write_b64 v13, v[220:221] offset:688\n\t"
    "v_mfma_f32_32x32x16_bf16 v[176:191], v[156:159], v[124:127], v[176:191]\n\t"
    "v_mul_f32 v204, v204, v226\n\t"
    "v_mul_f32 v205, v205, v226\n\t"
    "v_mul_f32 v206, v206, v226\n\t"
    "v_mul_f32 v207, v207, v226\n\t"
    "v_cvt_pk_bf16_f32 v222, v200, v201\n\t"
    "v_cvt_pk_bf16_f32 v223, v202, v203\n\t"
    "ds_write_b64 v13, v[222:223] offset:704\n\t"
    "v_cvt_pk_bf16_f32 v224, v204, v205\n\t"
    "v_cvt_pk_bf16_f32 v225, v206, v207\n\t"
    "s_mov_b32 exec_hi, 0\n\t"
    "ds_write_b64 v13, v[224:225] offset:720\n\t"
    "ds_write_b32 v15, v4 offset:344\n\t"
    "s_mov_b32 exec_hi, -1\n\t"
    "v_add_f32 v216, s24, v172\n\t"
    "v_mov_b32 v217, v216\n\t"
    "s_nop 1\n\t"
    "v_permlane32_swap_b32 v217, v216\n\t"
    "v_rcp_f32 v216, v216\n\t"
    "s_nop 0\n\t"
    "v_mul_f32 v160, v160, v216\n\t"
    "v_mul_f32 v161, v161, v216\n\t"
    "v_mul_f32 v162, v162, v216\n\t"
    "v_mul_f32 v163, v163, v216\n\t"
    "v_mul_f32 v164, v164, v216\n\t"
    "v_mul_f32 v165, v165, v216\n\t"
    "v_mul_f32 v166, v166, v216\n\t"
    "v_mul_f32 v167, v167, v216\n\t"
    "v_cvt_pk_bf16_f32 v208, v160, v161\n\t"
    "v_cvt_pk_bf16_f32 v209, v162, v163\n\t"
    "ds_write_b64 v13, v[208:209] offset:29632\n\t"
    "v_mul_f32 v168, v168, v216\n\t"
    "v_mul_f32 v169, v169, v216\n\t"
    "v_mul_f32 v170, v170, v216\n\t"
    "v_mul_f32 v171, v171, v216\n\t"
    "v_cvt_pk_bf16_f32 v210, v164, v165\n\t"
    "v_cvt_pk_bf16_f32 v211, v166, v167\n\t"
    "ds_write_b64 v13, v[210:211] offset:29648\n\t"
    "v_mul_f32 v172, v172, v216\n\t"
    "v_mul_f32 v173, v173, v216\n\t"
    "v_mul_f32 v174, v174, v216\n\t"
    "v_mul_f32 v175, v175, v216\n\t"
    "v_cvt_pk_bf16_f32 v212, v168, v169\n\t"
    "v_cvt_pk_bf16_f32 v213, v170, v171\n\t"
    "ds_write_b64 v13, v[212:213] offset:29664\n\t"
    "v_cvt_pk_bf16_f32 v214, v172, v173\n\t"
    "v_cvt_pk_bf16_f32 v215, v174, v175\n\t"
    "s_mov_b32 exec_hi, 0\n\t"
    "ds_write_b64 v13, v[214:215] offset:29680\n\t"
    "ds_write_b32 v15, v4 offset:360\n\t"
    "s_mov_b32 exec_hi, -1\n\t"
    "v_add_f32 v226, s24, v188\n\t"
    "v_mov_b32 v227, v226\n\t"
    "s_nop 1\n\t"
    "v_permlane32_swap_b32 v227, v226\n\t"
    "v_rcp_f32 v226, v226\n\t"
    "s_nop 0\n\t"
    "v_mul_f32 v176, v176, v226\n\t"
    "v_mul_f32 v177, v177, v226\n\t"
    "v_mul_f32 v178, v178, v226\n\t"
    "v_mul_f32 v179, v179, v226\n\t"
    "v_mul_f32 v180, v180, v226\n\t"
    "v_mul_f32 v181, v181, v226\n\t"
    "v_mul_f32 v182, v182, v226\n\t"
    "v_mul_f32 v183, v183, v226\n\t"
    "v_cvt_pk_bf16_f32 v218, v176, v177\n\t"
    "v_cvt_pk_bf16_f32 v219, v178, v179\n\t"
    "ds_write_b64 v13, v[218:219] offset:29856\n\t"
    "v_mul_f32 v184, v184, v226\n\t"
    "v_mul_f32 v185, v185, v226\n\t"
    "v_mul_f32 v186, v186, v226\n\t"
    "v_mul_f32 v187, v187, v226\n\t"
    "v_cvt_pk_bf16_f32 v220, v180, v181\n\t"
    "v_cvt_pk_bf16_f32 v221, v182, v183\n\t"
    "ds_write_b64 v13, v[220:221] offset:29872\n\t"
    "v_mul_f32 v188, v188, v226\n\t"
    "v_mul_f32 v189, v189, v226\n\t"
    "v_mul_f32 v190, v190, v226\n\t"
    "v_mul_f32 v191, v191, v226\n\t"
    "v_cvt_pk_bf16_f32 v222, v184, v185\n\t"
    "v_cvt_pk_bf16_f32 v223, v186, v187\n\t"
    "ds_write_b64 v13, v[222:223] offset:29888\n\t"
    "v_cvt_pk_bf16_f32 v224, v188, v189\n\t"
    "v_cvt_pk_bf16_f32 v225, v190, v191\n\t"
    "s_mov_b32 exec_hi, 0\n\t"
    "ds_write_b64 v13, v[224:225] offset:29904\n\t"
    "ds_write_b32 v15, v4 offset:376\n\t"
    "s_mov_b32 exec_hi, -1\n\t"
    "s_endpgm\n\t"
    ".Lsl47_%=0:\n\t"
    "v_or_b32 v26, v67, v71\n\t"
    "v_cmp_lt_u32 vcc, s27, v26\n\t"
    "s_cbranch_vccnz .Lsl47_%=f0\n\t"
    "s_add_u32 s30, s30, 32768\n\t"
    "s_min_u32 s30, s30, s23\n\t"
    "s_add_u32 s26, s30, 0\n\t"
    "buffer_load_dwordx4 v[128:131], v10, s[48:51], s26 offen offset:0 sc1\n\t"
    "s_add_u32 s26, s30, 0\n\t"
    "buffer_load_dwordx4 v[132:135], v10, s[48:51], s26 offen offset:1024 sc1\n\t"
    "s_add_u32 s26, s30, 8192\n\t"
    "buffer_load_dwordx4 v[136:139], v10, s[48:51], s26 offen offset:0 sc1\n\t"
    "s_add_u32 s26, s30, 8192\n\t"
    "buffer_load_dwordx4 v[140:143], v10, s[48:51], s26 offen offset:1024 sc1\n\t"
    "s_add_u32 s35, s35, 16\n\t"
    "s_cmp_lt_u32 s35, 0x400000\n\t"
    "s_waitcnt vmcnt(0)\n\t"
    "v_xor_b32 v131, v131, v18\n\t"
    "v_xor_b32 v135, v135, v18\n\t"
    "v_or_b32 v26, v131, v135\n\t"
    "v_cmp_lt_u32 vcc, s27, v26\n\t"
    "s_cselect_b64 vcc, vcc, 0\n\t"
    "s_cbranch_vccnz .Lsl47_%=0\n\t"
    "s_branch .Lsl47_%=r0\n\t"
    ".Lsl47_%=f0:\n\t"
    "s_add_u32 s30, s30, 32768\n\t"
    "s_min_u32 s30, s30, s23\n\t"
    "s_add_u32 s26, s31, 0\n\t"
    "buffer_load_dwordx4 v[64:67], v10, s[48:51], s26 offen offset:0 sc1\n\t"
    "s_add_u32 s26, s31, 0\n\t"
    "buffer_load_dwordx4 v[68:71], v10, s[48:51], s26 offen offset:1024 sc1\n\t"
    "s_add_u32 s26, s30, 0\n\t"
    "buffer_load_dwordx4 v[128:131], v10, s[48:51], s26 offen offset:0 sc1\n\t"
    "s_add_u32 s26, s30, 0\n\t"
    "buffer_load_dwordx4 v[132:135], v10, s[48:51], s26 offen offset:1024 sc1\n\t"
    "s_add_u32 s26, s31, 32768\n\t"
    "buffer_load_dwordx4 v[96:99], v10, s[48:51], s26 offen offset:0 sc1\n\t"
    "s_add_u32 s26, s31, 32768\n\t"
    "buffer_load_dwordx4 v[100:103], v10, s[48:51], s26 offen offset:1024 sc1\n\t"
    "s_add_u32 s26, s31, 8192\n\t"
    "buffer_load_dwordx4 v[72:75], v10, s[48:51], s26 offen offset:0 sc1\n\t"
    "s_add_u32 s26, s31, 8192\n\t"
    "buffer_load_dwordx4 v[76:79], v10, s[48:51], s26 offen offset:1024 sc1\n\t"
    "s_add_u32 s26, s30, 8192\n\t"
    "buffer_load_dwordx4 v[136:139], v10, s[48:51], s26 offen offset:0 sc1\n\t"
    "s_add_u32 s26, s30, 8192\n\t"
    "buffer_load_dwordx4 v[140:143], v10, s[48:51], s26 offen offset:1024 sc1\n\t"
    "s_add_u32 s26, s31, 40960\n\t"
    "buffer_load_dwordx4 v[104:107], v10, s[48:51], s26 offen offset:0 sc1\n\t"
    "s_add_u32 s26, s31, 40960\n\t"
    "buffer_load_dwordx4 v[108:111], v10, s[48:51], s26 offen offset:1024 sc1\n\t"
    "s_add_u32 s35, s35, 16\n\t"
    "s_cmp_lt_u32 s35, 0x400000\n\t"
    "s_waitcnt vmcnt(0)\n\t"
    "v_xor_b32 v67, v67, v18\n\t"
    "v_xor_b32 v71, v71, v18\n\t"
    "v_xor_b32 v131, v131, v18\n\t"
    "v_xor_b32 v135, v135, v18\n\t"
    "v_or_b32 v26, v67, v71\n\t"
    "v_or3_b32 v26, v26, v131, v135\n\t"
    "v_cmp_lt_u32 vcc, s27, v26\n\t"
    "s_cselect_b64 vcc, vcc, 0\n\t"
    "s_cbranch_vccnz .Lsl47_%=0\n\t"
    "s_branch .Lsl47_%=r0\n\t"
    ".Lsl47_%=1:\n\t"
    "s_add_u32 s30, s30, 32768\n\t"
    "s_min_u32 s30, s30, s23\n\t"
    "s_add_u32 s26, s31, 32768\n\t"
    "buffer_load_dwordx4 v[96:99], v10, s[48:51], s26 offen offset:0 sc1\n\t"
    "s_add_u32 s26, s31, 32768\n\t"
    "buffer_load_dwordx4 v[100:103], v10, s[48:51], s26 offen offset:1024 sc1\n\t"
    "s_add_u32 s26, s31, 8192\n\t"
    "buffer_load_dwordx4 v[72:75], v10, s[48:51], s26 offen offset:0 sc1\n\t"
    "s_add_u32 s26, s31, 8192\n\t"
    "buffer_load_dwordx4 v[76:79], v10, s[48:51], s26 offen offset:1024 sc1\n\t"
    "s_add_u32 s26, s30, 8192\n\t"
    "buffer_load_dwordx4 v[136:139], v10, s[48:51], s26 offen offset:0 sc1\n\t"
    "s_add_u32 s26, s30, 8192\n\t"
    "buffer_load_dwordx4 v[140:143], v10, s[48:51], s26 offen offset:1024 sc1\n\t"
    "s_add_u32 s26, s31, 40960\n\t"
    "buffer_load_dwordx4 v[104:107], v10, s[48:51], s26 offen offset:0 sc1\n\t"
    "s_add_u32 s26, s31, 40960\n\t"
    "buffer_load_dwordx4 v[108:111], v10, s[48:51], s26 offen offset:1024 sc1\n\t"
    "s_add_u32 s26, s31, 16384\n\t"
    "buffer_load_dwordx4 v[80:83], v10, s[48:51], s26 offen offset:0 sc1\n\t"
    "s_add_u32 s26, s31, 16384\n\t"
    "buffer_load_dwordx4 v[84:87], v10, s[48:51], s26 offen offset:1024 sc1\n\t"
    "s_add_u32 s26, s30, 16384\n\t"
    "buffer_load_dwordx4 v[144:147], v10, s[48:51], s26 offen offset:0 sc1\n\t"
    "s_add_u32 s26, s30, 16384\n\t"
    "buffer_load_dwordx4 v[148:151], v10, s[48:51], s26 offen offset:1024 sc1\n\t"
    "s_add_u32 s35, s35, 16\n\t"
    "s_cmp_lt_u32 s35, 0x400000\n\t"
    "s_waitcnt vmcnt(0)\n\t"
    "v_xor_b32 v99, v99, v18\n\t"
    "v_xor_b32 v103, v103, v18\n\t"
    "v_or_b32 v26, v99, v103\n\t"
    "v_cmp_lt_u32 vcc, s27, v26\n\t"
    "s_cselect_b64 vcc, vcc, 0\n\t"
    "s_cbranch_vccnz .Lsl47_%=1\n\t"
    "s_branch .Lsl47_%=r1\n\t"
    ".Lsl47_%=2:\n\t"
    "v_or_b32 v26, v75, v79\n\t"
    "v_cmp_lt_u32 vcc, s27, v26\n\t"
    "s_cbranch_vccnz .Lsl47_%=f2\n\t"
    "s_add_u32 s30, s30, 32768\n\t"
    "s_min_u32 s30, s30, s23\n\t"
    "s_add_u32 s26, s30, 8192\n\t"
    "buffer_load_dwordx4 v[136:139], v10, s[48:51], s26 offen offset:0 sc1\n\t"
    "s_add_u32 s26, s30, 8192\n\t"
    "buffer_load_dwordx4 v[140:143], v10, s[48:51], s26 offen offset:1024 sc1\n\t"
    "s_add_u32 s26, s30, 16384\n\t"
    "buffer_load_dwordx4 v[144:147], v10, s[48:51], s26 offen offset:0 sc1\n\t"
    "s_add_u32 s26, s30, 16384\n\t"
    "buffer_load_dwordx4 v[148:151], v10, s[48:51], s26 offen offset:1024 sc1\n\t"
    "s_add_u32 s26, s30, 24576\n\t"
    "buffer_load_dwordx4 v[152:155], v10, s[48:51], s26 offen offset:0 sc1\n\t"
    "s_add_u32 s26, s30, 24576\n\t"
    "buffer_load_dwordx4 v[156:159], v10, s[48:51], s26 offen offset:1024 sc1\n\t"
    "s_add_u32 s35, s35, 16\n\t"
    "s_cmp_lt_u32 s35, 0x400000\n\t"
    "s_waitcnt vmcnt(0)\n\t"
    "v_xor_b32 v139, v139, v18\n\t"
    "v_xor_b32 v143, v143, v18\n\t"
    "v_or_b32 v26, v139, v143\n\t"
    "v_cmp_lt_u32 vcc, s27, v26\n\t"
    "s_cselect_b64 vcc, vcc, 0\n\t"
    "s_cbranch_vccnz .Lsl47_%=2\n\t"
    "s_branch .Lsl47_%=r2\n\t"
    ".Lsl47_%=f2:\n\t"
    "s_add_u32 s30, s30, 32768\n\t"
    "s_min_u32 s30, s30, s23\n\t"
    "s_add_u32 s26, s31, 8192\n\t"
    "buffer_load_dwordx4 v[72:75], v10, s[48:51], s26 offen offset:0 sc1\n\t"
    "s_add_u32 s26, s31, 8192\n\t"
    "buffer_load_dwordx4 v[76:79], v10, s[48:51], s26 offen offset:1024 sc1\n\t"
    "s_add_u32 s26, s30, 8192\n\t"
    "buffer_load_dwordx4 v[136:139], v10, s[48:51], s26 offen offset:0 sc1\n\t"
    "s_add_u32 s26, s30, 8192\n\t"
    "buffer_load_dwordx4 v[140:143], v10, s[48:51], s26 offen offset:1024 sc1\n\t"
    "s_add_u32 s26, s31, 40960\n\t"
    "buffer_load_dwordx4 v[104:107], v10, s[48:51], s26 offen offset:0 sc1\n\t"
    "s_add_u32 s26, s31, 40960\n\t"
    "buffer_load_dwordx4 v[108:111], v10, s[48:51], s26 offen offset:1024 sc1\n\t"
    "s_add_u32 s26, s31, 16384\n\t"
    "buffer_load_dwordx4 v[80:83], v10, s[48:51], s26 offen offset:0 sc1\n\t"
    "s_add_u32 s26, s31, 16384\n\t"
    "buffer_load_dwordx4 v[84:87], v10, s[48:51], s26 offen offset:1024 sc1\n\t"
    "s_add_u32 s26, s30, 16384\n\t"
    "buffer_load_dwordx4 v[144:147], v10, s[48:51], s26 offen offset:0 sc1\n\t"
    "s_add_u32 s26, s30, 16384\n\t"
    "buffer_load_dwordx4 v[148:151], v10, s[48:51], s26 offen offset:1024 sc1\n\t"
    "s_add_u32 s26, s31, 24576\n\t"
    "buffer_load_dwordx4 v[88:91], v10, s[48:51], s26 offen offset:0 sc1\n\t"
    "s_add_u32 s26, s31, 24576\n\t"
    "buffer_load_dwordx4 v[92:95], v10, s[48:51], s26 offen offset:1024 sc1\n\t"
    "s_add_u32 s26, s30, 24576\n\t"
    "buffer_load_dwordx4 v[152:155], v10, s[48:51], s26 offen offset:0 sc1\n\t"
    "s_add_u32 s26, s30, 24576\n\t"
    "buffer_load_dwordx4 v[156:159], v10, s[48:51], s26 offen offset:1024 sc1\n\t"
    "s_add_u32 s35, s35, 16\n\t"
    "s_cmp_lt_u32 s35, 0x400000\n\t"
    "s_waitcnt vmcnt(0)\n\t"
    "v_xor_b32 v75, v75, v18\n\t"
    "v_xor_b32 v79, v79, v18\n\t"
    "v_xor_b32 v139, v139, v18\n\t"
    "v_xor_b32 v143, v143, v18\n\t"
    "v_or_b32 v26, v75, v79\n\t"
    "v_or3_b32 v26, v26, v139, v143\n\t"
    "v_cmp_lt_u32 vcc, s27, v26\n\t"
    "s_cselect_b64 vcc, vcc, 0\n\t"
    "s_cbranch_vccnz .Lsl47_%=2\n\t"
    "s_branch .Lsl47_%=r2\n\t"
    ".Lsl47_%=3:\n\t"
    "s_add_u32 s30, s30, 32768\n\t"
    "s_min_u32 s30, s30, s23\n\t"
    "s_add_u32 s26, s31, 40960\n\t"
    "buffer_load_dwordx4 v[104:107], v10, s[48:51], s26 offen offset:0 sc1\n\t"
    "s_add_u32 s26, s31, 40960\n\t"
    "buffer_load_dwordx4 v[108:111], v10, s[48:51], s26 offen offset:1024 sc1\n\t"
    "s_add_u32 s26, s31, 16384\n\t"
    "buffer_load_dwordx4 v[80:83], v10, s[48:51], s26 offen offset:0 sc1\n\t"
    "s_add_u32 s26, s31, 16384\n\t"
    "buffer_load_dwordx4 v[84:87], v10, s[48:51], s26 offen offset:1024 sc1\n\t"
    "s_add_u32 s26, s30, 16384\n\t"
    "buffer_load_dwordx4 v[144:147], v10, s[48:51], s26 offen offset:0 sc1\n\t"
    "s_add_u32 s26, s30, 16384\n\t"
    "buffer_load_dwordx4 v[148:151], v10, s[48:51], s26 offen offset:1024 sc1\n\t"
    "s_add_u32 s26, s31, 24576\n\t"
    "buffer_load_dwordx4 v[88:91], v10, s[48:51], s26 offen offset:0 sc1\n\t"
    "s_add_u32 s26, s31, 24576\n\t"
    "buffer_load_dwordx4 v[92:95], v10, s[48:51], s26 offen offset:1024 sc1\n\t"
    "s_add_u32 s26, s30, 24576\n\t"
    "buffer_load_dwordx4 v[152:155], v10, s[48:51], s26 offen offset:0 sc1\n\t"
    "s_add_u32 s26, s30, 24576\n\t"
    "buffer_load_dwordx4 v[156:159], v10, s[48:51], s26 offen offset:1024 sc1\n\t"
    "s_add_u32 s26, s31, 49152\n\t"
    "buffer_load_dwordx4 v[112:115], v10, s[48:51], s26 offen offset:0 sc1\n\t"
    "s_add_u32 s26, s31, 49152\n\t"
    "buffer_load_dwordx4 v[116:119], v10, s[48:51], s26 offen offset:1024 sc1\n\t"
    "s_add_u32 s26, s31, 57344\n\t"
    "buffer_load_dwordx4 v[120:123], v10, s[48:51], s26 offen offset:0 sc1\n\t"
    "s_add_u32 s26, s31, 57344\n\t"
    "buffer_load_dwordx4 v[124:127], v10, s[48:51], s26 offen offset:1024 sc1\n\t"
    "s_add_u32 s35, s35, 16\n\t"
    "s_cmp_lt_u32 s35, 0x400000\n\t"
    "s_waitcnt vmcnt(0)\n\t"
    "v_xor_b32 v107, v107, v18\n\t"
    "v_xor_b32 v111, v111, v18\n\t"
    "v_or_b32 v26, v107, v111\n\t"
    "v_cmp_lt_u32 vcc, s27, v26\n\t"
    "s_cselect_b64 vcc, vcc, 0\n\t"
    "s_cbranch_vccnz .Lsl47_%=3\n\t"
    "s_branch .Lsl47_%=r3\n\t"
    ".Lsl47_%=4:\n\t"
    "v_or_b32 v26, v83, v87\n\t"
    "v_cmp_lt_u32 vcc, s27, v26\n\t"
    "s_cbranch_vccnz .Lsl47_%=f4\n\t"
    "s_add_u32 s30, s30, 32768\n\t"
    "s_min_u32 s30, s30, s23\n\t"
    "s_add_u32 s26, s30, 16384\n\t"
    "buffer_load_dwordx4 v[144:147], v10, s[48:51], s26 offen offset:0 sc1\n\t"
    "s_add_u32 s26, s30, 16384\n\t"
    "buffer_load_dwordx4 v[148:151], v10, s[48:51], s26 offen offset:1024 sc1\n\t"
    "s_add_u32 s26, s30, 24576\n\t"
    "buffer_load_dwordx4 v[152:155], v10, s[48:51], s26 offen offset:0 sc1\n\t"
    "s_add_u32 s26, s30, 24576\n\t"
    "buffer_load_dwordx4 v[156:159], v10, s[48:51], s26 offen offset:1024 sc1\n\t"
    "s_add_u32 s35, s35, 16\n\t"
    "s_cmp_lt_u32 s35, 0x400000\n\t"
    "s_waitcnt vmcnt(0)\n\t"
    "v_xor_b32 v147, v147, v18\n\t"
    "v_xor_b32 v151, v151, v18\n\t"
    "v_or_b32 v26, v147, v151\n\t"
    "v_cmp_lt_u32 vcc, s27, v26\n\t"
    "s_cselect_b64 vcc, vcc, 0\n\t"
    "s_cbranch_vccnz .Lsl47_%=4\n\t"
    "s_branch .Lsl47_%=r4\n\t"
    ".Lsl47_%=f4:\n\t"
    "s_add_u32 s30, s30, 32768\n\t"
    "s_min_u32 s30, s30, s23\n\t"
    "s_add_u32 s26, s31, 16384\n\t"
    "buffer_load_dwordx4 v[80:83], v10, s[48:51], s26 offen offset:0 sc1\n\t"
    "s_add_u32 s26, s31, 16384\n\t"
    "buffer_load_dwordx4 v[84:87], v10, s[48:51], s26 offen offset:1024 sc1\n\t"
    "s_add_u32 s26, s30, 16384\n\t"
    "buffer_load_dwordx4 v[144:147], v10, s[48:51], s26 offen offset:0 sc1\n\t"
    "s_add_u32 s26, s30, 16384\n\t"
    "buffer_load_dwordx4 v[148:151], v10, s[48:51], s26 offen offset:1024 sc1\n\t"
    "s_add_u32 s26, s31, 24576\n\t"
    "buffer_load_dwordx4 v[88:91], v10, s[48:51], s26 offen offset:0 sc1\n\t"
    "s_add_u32 s26, s31, 24576\n\t"
    "buffer_load_dwordx4 v[92:95], v10, s[48:51], s26 offen offset:1024 sc1\n\t"
    "s_add_u32 s26, s30, 24576\n\t"
    "buffer_load_dwordx4 v[152:155], v10, s[48:51], s26 offen offset:0 sc1\n\t"
    "s_add_u32 s26, s30, 24576\n\t"
    "buffer_load_dwordx4 v[156:159], v10, s[48:51], s26 offen offset:1024 sc1\n\t"
    "s_add_u32 s26, s31, 49152\n\t"
    "buffer_load_dwordx4 v[112:115], v10, s[48:51], s26 offen offset:0 sc1\n\t"
    "s_add_u32 s26, s31, 49152\n\t"
    "buffer_load_dwordx4 v[116:119], v10, s[48:51], s26 offen offset:1024 sc1\n\t"
    "s_add_u32 s26, s31, 57344\n\t"
    "buffer_load_dwordx4 v[120:123], v10, s[48:51], s26 offen offset:0 sc1\n\t"
    "s_add_u32 s26, s31, 57344\n\t"
    "buffer_load_dwordx4 v[124:127], v10, s[48:51], s26 offen offset:1024 sc1\n\t"
    "s_add_u32 s35, s35, 16\n\t"
    "s_cmp_lt_u32 s35, 0x400000\n\t"
    "s_waitcnt vmcnt(0)\n\t"
    "v_xor_b32 v83, v83, v18\n\t"
    "v_xor_b32 v87, v87, v18\n\t"
    "v_xor_b32 v147, v147, v18\n\t"
    "v_xor_b32 v151, v151, v18\n\t"
    "v_or_b32 v26, v83, v87\n\t"
    "v_or3_b32 v26, v26, v147, v151\n\t"
    "v_cmp_lt_u32 vcc, s27, v26\n\t"
    "s_cselect_b64 vcc, vcc, 0\n\t"
    "s_cbranch_vccnz .Lsl47_%=4\n\t"
    "s_branch .Lsl47_%=r4\n\t"
    ".Lsl47_%=5:\n\t"
    "v_or_b32 v26, v91, v95\n\t"
    "v_cmp_lt_u32 vcc, s27, v26\n\t"
    "s_cbranch_vccnz .Lsl47_%=f5\n\t"
    "s_add_u32 s30, s30, 32768\n\t"
    "s_min_u32 s30, s30, s23\n\t"
    "s_add_u32 s26, s30, 24576\n\t"
    "buffer_load_dwordx4 v[152:155], v10, s[48:51], s26 offen offset:0 sc1\n\t"
    "s_add_u32 s26, s30, 24576\n\t"
    "buffer_load_dwordx4 v[156:159], v10, s[48:51], s26 offen offset:1024 sc1\n\t"
    "s_add_u32 s35, s35, 16\n\t"
    "s_cmp_lt_u32 s35, 0x400000\n\t"
    "s_waitcnt vmcnt(0)\n\t"
    "v_xor_b32 v155, v155, v18\n\t"
    "v_xor_b32 v159, v159, v18\n\t"
    "v_or_b32 v26, v155, v159\n\t"
    "v_cmp_lt_u32 vcc, s27, v26\n\t"
    "s_cselect_b64 vcc, vcc, 0\n\t"
    "s_cbranch_vccnz .Lsl47_%=5\n\t"
    "s_branch .Lsl47_%=r5\n\t"
    ".Lsl47_%=f5:\n\t"
    "s_add_u32 s30, s30, 32768\n\t"
    "s_min_u32 s30, s30, s23\n\t"
    "s_add_u32 s26, s31, 24576\n\t"
    "buffer_load_dwordx4 v[88:91], v10, s[48:51], s26 offen offset:0 sc1\n\t"
    "s_add_u32 s26, s31, 24576\n\t"
    "buffer_load_dwordx4 v[92:95], v10, s[48:51], s26 offen offset:1024 sc1\n\t"
    "s_add_u32 s26, s30, 24576\n\t"
    "buffer_load_dwordx4 v[152:155], v10, s[48:51], s26 offen offset:0 sc1\n\t"
    "s_add_u32 s26, s30, 24576\n\t"
    "buffer_load_dwordx4 v[156:159], v10, s[48:51], s26 offen offset:1024 sc1\n\t"
    "s_add_u32 s26, s31, 49152\n\t"
    "buffer_load_dwordx4 v[112:115], v10, s[48:51], s26 offen offset:0 sc1\n\t"
    "s_add_u32 s26, s31, 49152\n\t"
    "buffer_load_dwordx4 v[116:119], v10, s[48:51], s26 offen offset:1024 sc1\n\t"
    "s_add_u32 s26, s31, 57344\n\t"
    "buffer_load_dwordx4 v[120:123], v10, s[48:51], s26 offen offset:0 sc1\n\t"
    "s_add_u32 s26, s31, 57344\n\t"
    "buffer_load_dwordx4 v[124:127], v10, s[48:51], s26 offen offset:1024 sc1\n\t"
    "s_add_u32 s35, s35, 16\n\t"
    "s_cmp_lt_u32 s35, 0x400000\n\t"
    "s_waitcnt vmcnt(0)\n\t"
    "v_xor_b32 v91, v91, v18\n\t"
    "v_xor_b32 v95, v95, v18\n\t"
    "v_xor_b32 v155, v155, v18\n\t"
    "v_xor_b32 v159, v159, v18\n\t"
    "v_or_b32 v26, v91, v95\n\t"
    "v_or3_b32 v26, v26, v155, v159\n\t"
    "v_cmp_lt_u32 vcc, s27, v26\n\t"
    "s_cselect_b64 vcc, vcc, 0\n\t"
    "s_cbranch_vccnz .Lsl47_%=5\n\t"
    "s_branch .Lsl47_%=r5\n\t"
    ".Lsl47_%=6:\n\t"
    "s_add_u32 s26, s31, 49152\n\t"
    "buffer_load_dwordx4 v[112:115], v10, s[48:51], s26 offen offset:0 sc1\n\t"
    "s_add_u32 s26, s31, 49152\n\t"
    "buffer_load_dwordx4 v[116:119], v10, s[48:51], s26 offen offset:1024 sc1\n\t"
    "s_add_u32 s26, s31, 57344\n\t"
    "buffer_load_dwordx4 v[120:123], v10, s[48:51], s26 offen offset:0 sc1\n\t"
    "s_add_u32 s26, s31, 57344\n\t"
    "buffer_load_dwordx4 v[124:127], v10, s[48:51], s26 offen offset:1024 sc1\n\t"
    "s_add_u32 s35, s35, 16\n\t"
    "s_cmp_lt_u32 s35, 0x400000\n\t"
    "s_waitcnt vmcnt(0)\n\t"
    "v_xor_b32 v115, v115, v18\n\t"
    "v_xor_b32 v119, v119, v18\n\t"
    "v_or_b32 v26, v115, v119\n\t"
    "v_cmp_lt_u32 vcc, s27, v26\n\t"
    "s_cselect_b64 vcc, vcc, 0\n\t"
    "s_cbranch_vccnz .Lsl47_%=6\n\t"
    "s_branch .Lsl47_%=r6\n\t"
    ".Lsl47_%=7:\n\t"
    "s_add_u32 s26, s31, 57344\n\t"
    "buffer_load_dwordx4 v[120:123], v10, s[48:51], s26 offen offset:0 sc1\n\t"
    "s_add_u32 s26, s31, 57344\n\t"
    "buffer_load_dwordx4 v[124:127], v10, s[48:51], s26 offen offset:1024 sc1\n\t"
    "s_add_u32 s35, s35, 16\n\t"
    "s_cmp_lt_u32 s35, 0x400000\n\t"
    "s_waitcnt vmcnt(0)\n\t"
    "v_xor_b32 v123, v123, v18\n\t"
    "v_xor_b32 v127, v127, v18\n\t"
    "v_or_b32 v26, v123, v127\n\t"
    "v_cmp_lt_u32 vcc, s27, v26\n\t"
    "s_cselect_b64 vcc, vcc, 0\n\t"
    "s_cbranch_vccnz .Lsl47_%=7\n\t"
    "s_branch .Lsl47_%=r7\n\t"
    ".LC2_%=:\n\t"
    "s_and_b32 s14, s8, 3\n\t"
    "s_setprio 3\n\t"
    "v_lshrrev_b32 v2, 5, v1\n\t"
    "v_and_b32 v3, 31, v1\n\t"
    "s_and_b32 s15, s8, 1\n\t"
    "s_bfe_u32 s16, s8, 0x10001\n\t"
    "v_and_b32 v30, 3, v10\n\t"
    "v_bfe_u32 v31, v1, 4, 1\n\t"
    "v_bfe_u32 v32, v1, 1, 3\n\t"
    "v_lshl_add_u32 v33, s16, 6, v3\n\t"
    "v_xor_b32 v32, v32, v2\n\t"
    "v_lshl_add_u32 v31, v31, 2, v7\n\t"
    "v_lshlrev_b32 v33, 7, v33\n\t"
    "s_mul_i32 s25, s15, 7\n\t"
    "s_waitcnt lgkmcnt(0)\n\t"
    "s_load_dwordx2 s[78:79], s[48:49], 0x0\n\t"
    "v_lshl_add_u32 v4, v32, 4, v33\n\t"
    "v_add_u32 v32, s25, v31\n\t"
    "v_lshl_add_u32 v33, v30, 1, v2\n\t"
    "v_xor_b32 v5, 32, v4\n\t"
    "v_xor_b32 v6, 64, v4\n\t"
    "v_xor_b32 v8, 0x60, v4\n\t"
    "v_mul_u32_u24 v33, 0x1e0, v33\n\t"
    "v_cmp_eq_u32 vcc, 7, v31\n\t"
    "s_sub_u32 s25, 15, s15\n\t"
    "v_add_u32 v34, 16, v32\n\t"
    "v_mov_b32 v35, s25\n\t"
    "v_add_u32 v33, 66560, v33\n\t"
    "v_cndmask_b32 v32, v32, v35, vcc\n\t"
    "v_cndmask_b32 v34, v34, v35, vcc\n\t"
    "v_add_u32 v17, 0x10000, v4\n\t"
    "v_add_u32 v18, 0x10000, v5\n\t"
    "v_add_u32 v19, 0x10000, v6\n\t"
    "v_add_u32 v20, 0x10000, v8\n\t"
    "v_lshl_add_u32 v10, v32, 3, v33\n\t"
    "v_lshl_add_u32 v11, v34, 3, v33\n\t"
    "v_and_b32 v12, 1, v1\n\t"
    "v_cmp_eq_u32 vcc, 28, v3\n\t"
    "v_add_u32 v21, 32768, v10\n\t"
    "v_add_u32 v7, 32768, v11\n\t"
    "v_lshlrev_b32 v12, 2, v12\n\t"
    "s_mov_b32 s84, 1.0\n\t"
    "v_cndmask_b32 v24, 0, 1.0, vcc\n\t"
    "v_add_u32 v12, 160832, v12\n\t"
    "s_mov_b32 s85, 1.0\n\t"
    "v_mov_b32 v25, v24\n\t"
    "s_lshl2_add_u32 s17, s14, 161072\n\t"
    "v_mov_b64 v[46:47], s[84:85]\n\t"
    "v_mov_b64 v[62:63], v[24:25]\n\t"
    "v_mov_b64 v[110:111], s[84:85]\n\t"
    "v_mov_b64 v[48:49], s[84:85]\n\t"
    "v_mov_b64 v[64:65], v[24:25]\n\t"
    "v_mov_b64 v[112:113], s[84:85]\n\t"
    "v_mov_b64 v[50:51], s[84:85]\n\t"
    "v_mov_b64 v[66:67], v[24:25]\n\t"
    "v_mov_b64 v[114:115], s[84:85]\n\t"
    "v_mov_b64 v[52:53], s[84:85]\n\t"
    "v_mov_b64 v[68:69], v[24:25]\n\t"
    "v_mov_b64 v[116:117], s[84:85]\n\t"
    "v_mov_b64 v[54:55], s[84:85]\n\t"
    "v_mov_b64 v[70:71], v[24:25]\n\t"
    "v_mov_b64 v[118:119], s[84:85]\n\t"
    "v_mov_b64 v[56:57], s[84:85]\n\t"
    "v_mov_b64 v[72:73], v[24:25]\n\t"
    "v_mov_b64 v[120:121], s[84:85]\n\t"
    "v_mov_b64 v[58:59], s[84:85]\n\t"
    "v_mov_b64 v[74:75], v[24:25]\n\t"
    "v_mov_b64 v[122:123], s[84:85]\n\t"
    "v_mov_b64 v[60:61], s[84:85]\n\t"
    "v_mov_b64 v[76:77], v[24:25]\n\t"
    "v_mov_b64 v[124:125], s[84:85]\n\t"
    "v_mov_b64 v[78:79], s[84:85]\n\t"
    "v_mov_b64 v[94:95], v[24:25]\n\t"
    "v_mov_b64 v[126:127], s[84:85]\n\t"
    "v_mov_b64 v[80:81], s[84:85]\n\t"
    "v_mov_b64 v[96:97], v[24:25]\n\t"
    "v_mov_b64 v[128:129], s[84:85]\n\t"
    "v_mov_b64 v[82:83], s[84:85]\n\t"
    "v_mov_b64 v[98:99], v[24:25]\n\t"
    "v_mov_b64 v[130:131], s[84:85]\n\t"
    "v_mov_b64 v[84:85], s[84:85]\n\t"
    "v_mov_b64 v[100:101], v[24:25]\n\t"
    "v_mov_b64 v[132:133], s[84:85]\n\t"
    "v_mov_b64 v[86:87], s[84:85]\n\t"
    "v_mov_b64 v[102:103], v[24:25]\n\t"
    "v_mov_b64 v[134:135], s[84:85]\n\t"
    "v_mov_b64 v[88:89], s[84:85]\n\t"
    "v_mov_b64 v[104:105], v[24:25]\n\t"
    "v_mov_b64 v[136:137], s[84:85]\n\t"
    "v_mov_b64 v[90:91], s[84:85]\n\t"
    "v_mov_b64 v[106:107], v[24:25]\n\t"
    "v_mov_b64 v[138:139], s[84:85]\n\t"
    "v_mov_b64 v[92:93], s[84:85]\n\t"
    "v_mov_b64 v[108:109], v[24:25]\n\t"
    "v_mov_b64 v[140:141], s[84:85]\n\t"
    "s_lshl1_add_u32 s24, s12, s15\n\t"
    "s_lshl1_add_u32 s25, s11, s16\n\t"
    "s_lshl_b32 s28, s24, 8\n\t"
    "s_add_u32 s28, s28, s25\n\t"
    "s_lshl_b32 s28, s28, 8\n\t"
    "s_add_u32 s28, s28, 184320\n\t"
    "s_add_u32 s29, s28, 32768\n\t"
    "s_lshl_b32 s30, s25, 5\n\t"
    "s_add_u32 s30, s30, s24\n\t"
    "s_lshl_b32 s30, s30, 11\n\t"
    "s_add_u32 s30, s30, 1232896\n\t"
    "s_add_u32 s31, s30, 32768\n\t"
    "v_lshrrev_b32 v30, 4, v1\n\t"
    "v_and_b32 v31, 15, v1\n\t"
    "v_lshlrev_b32 v15, 4, v1\n\t"
    "v_lshlrev_b32 v31, 4, v31\n\t"
    "v_lshl_add_u32 v14, v30, 13, v31\n\t"
    "s_mov_b32 s25, 0x3fb8aa3b\n\t"
    "s_mov_b32 s26, 0x358637bd\n\t"
    "s_mov_b32 s27, 0x358637bd\n\t"
    "s_waitcnt vmcnt(0)\n\t"
    "s_getpc_b64 s[18:19]\n\t"
    ".Lgo49_%=x:\n\t"
    "s_add_u32 s18, s18, .Lgo49_%=-.Lgo49_%=x\n\t"
    "s_addc_u32 s19, s19, 0\n\t"
    ".Lsp48_%=:\n\t"
    "ds_read_b32 v13, v12\n\t"
    "s_add_u32 s35, s35, 1\n\t"
    "s_cmp_lt_u32 s35, 0x400000\n\t"
    "s_waitcnt lgkmcnt(0)\n\t"
    "v_cmp_ne_u32 vcc, 0, v13\n\t"
    "s_cselect_b64 vcc, vcc, 0\n\t"
    "s_cbranch_vccnz .Lsp48_%=\n\t"
    "s_setpc_b64 s[18:19]\n\t"
    ".Lgo49_%=:\n\t"
    "ds_read_b64_tr_b16 v[174:175], v10 offset:0\n\t"
    "ds_read_b64_tr_b16 v[176:177], v10 offset:240\n\t"
    "ds_read_b128 v[142:145], v17 offset:32768\n\t"
    "ds_read_b64_tr_b16 v[178:179], v11 offset:0\n\t"
    "ds_read_b64_tr_b16 v[180:181], v11 offset:240\n\t"
    "ds_read_b128 v[146:149], v17 offset:36864\n\t"
    "ds_read_b64_tr_b16 v[182:183], v10 offset:3840\n\t"
    "ds_read_b64_tr_b16 v[184:185], v10 offset:4080\n\t"
    "ds_read_b128 v[150:153], v18 offset:32768\n\t"
    "v_add_u32 v12, 8, v12\n\t"
    "ds_read_b32 v13, v12\n\t"
    "s_waitcnt lgkmcnt(7)\n\t"
    "v_mfma_f32_32x32x16_bf16 v[46:61], v[142:145], v[174:177], v[46:61]\n\t"
    "ds_read_b64_tr_b16 v[186:187], v11 offset:3840\n\t"
    "ds_read_b64_tr_b16 v[188:189], v11 offset:4080\n\t"
    "ds_read_b128 v[154:157], v18 offset:36864\n\t"
    "s_waitcnt lgkmcnt(8)\n\t"
    "v_mfma_f32_32x32x16_bf16 v[62:77], v[142:145], v[178:181], v[62:77]\n\t"
    "s_waitcnt lgkmcnt(3)\n\t"
    "v_cmp_ne_u32 vcc, 0, v13\n\t"
    "v_mfma_f32_32x32x16_bf16 v[78:93], v[146:149], v[174:177], v[78:93]\n\t"
    "s_getpc_b64 s[18:19]\n\t"
    "s_cbranch_vccnz .Lsp48_%=\n\t"
    "ds_read_b64_tr_b16 v[190:191], v10 offset:7680\n\t"
    "ds_read_b64_tr_b16 v[192:193], v10 offset:7920\n\t"
    "ds_read_b128 v[158:161], v19 offset:32768\n\t"
    "v_mfma_f32_32x32x16_bf16 v[94:109], v[146:149], v[178:181], v[94:109]\n\t"
    "ds_read_b64_tr_b16 v[194:195], v11 offset:7680\n\t"
    "ds_read_b64_tr_b16 v[196:197], v11 offset:7920\n\t"
    "v_mfma_f32_32x32x16_bf16 v[46:61], v[150:153], v[182:185], v[46:61]\n\t"
    "ds_read_b128 v[162:165], v19 offset:36864\n\t"
    "s_add_u32 s78, s78, 0x7f4a7c15\n\t"
    "s_addc_u32 s79, s79, 0x9e3779b9\n\t"
    "v_lshlrev_b32 v22, 4, v1\n\t"
    "v_lshrrev_b64 v[22:23], v22, s[78:79]\n\t"
    "v_lshlrev_b32 v22, 16, v22\n\t"
    "v_mov_b32 v16, v22\n\t"
    "s_waitcnt lgkmcnt(7)\n\t"
    "v_mfma_f32_32x32x16_bf16 v[62:77], v[150:153], v[186:189], v[62:77]\n\t"
    "v_mov_b32 v252, s17\n\t"
    "v_mov_b32 v253, 0\n\t"
    "s_waitcnt lgkmcnt(6)\n\t"
    "v_mfma_f32_32x32x16_bf16 v[78:93], v[154:157], v[182:185], v[78:93]\n\t"
    "ds_read_b64_tr_b16 v[198:199], v10 offset:11520\n\t"
    "ds_read_b64_tr_b16 v[200:201], v10 offset:11760\n\t"
    "ds_read_b128 v[166:169], v20 offset:32768\n\t"
    "v_mfma_f32_32x32x16_bf16 v[94:109], v[154:157], v[186:189], v[94:109]\n\t"
    "v_add_u32 v12, 8, v12\n\t"
    "ds_read_b32 v13, v12\n\t"
    "ds_read_b64_tr_b16 v[202:203], v11 offset:11520\n\t"
    "ds_read_b64_tr_b16 v[204:205], v11 offset:11760\n\t"
    "s_waitcnt lgkmcnt(9)\n\t"
    "v_mfma_f32_32x32x16_bf16 v[46:61], v[158:161], v[190:193], v[46:61]\n\t"
    "ds_read_b128 v[170:173], v20 offset:36864\n\t"
    "s_waitcnt lgkmcnt(8)\n\t"
    "v_mfma_f32_32x32x16_bf16 v[62:77], v[158:161], v[194:197], v[62:77]\n\t"
    "s_waitcnt lgkmcnt(3)\n\t"
    "v_cmp_ne_u32 vcc, 0, v13\n\t"
    "v_mfma_f32_32x32x16_bf16 v[78:93], v[162:165], v[190:193], v[78:93]\n\t"
    "s_getpc_b64 s[18:19]\n\t"
    "s_cbranch_vccnz .Lsp48_%=\n\t"
    "ds_read_b64_tr_b16 v[206:207], v10 offset:15360\n\t"
    "ds_read_b64_tr_b16 v[208:209], v10 offset:15600\n\t"
    "ds_read_b128 v[210:213], v4 offset:0\n\t"
    "v_mfma_f32_32x32x16_bf16 v[94:109], v[162:165], v[194:197], v[94:109]\n\t"
    "ds_read_b64_tr_b16 v[214:215], v11 offset:15360\n\t"
    "ds_read_b64_tr_b16 v[216:217], v11 offset:15600\n\t"
    "v_mfma_f32_32x32x16_bf16 v[46:61], v[166:169], v[198:201], v[46:61]\n\t"
    "ds_read_b128 v[218:221], v4 offset:4096\n\t"
    "s_waitcnt lgkmcnt(7)\n\t"
    "v_mfma_f32_32x32x16_bf16 v[62:77], v[166:169], v[202:205], v[62:77]\n\t"
    "s_waitcnt lgkmcnt(6)\n\t"
    "v_mfma_f32_32x32x16_bf16 v[78:93], v[170:173], v[198:201], v[78:93]\n\t"
    "ds_read_b64_tr_b16 v[222:223], v10 offset:19200\n\t"
    "ds_read_b64_tr_b16 v[224:225], v10 offset:19440\n\t"
    "ds_read_b128 v[226:229], v5 offset:0\n\t"
    "s_add_u32 m0, s17, 0\n\t"
    "s_mov_b64 exec, 1\n\t"
    "buffer_load_dword v9, s[36:39], 0 offen lds\n\t"
    "s_mov_b64 exec, -1\n\t"
    "v_mfma_f32_32x32x16_bf16 v[94:109], v[170:173], v[202:205], v[94:109]\n\t"
    "v_add_u32 v12, 8, v12\n\t"
    "ds_read_b32 v13, v12\n\t"
    "ds_read_b64_tr_b16 v[230:231], v11 offset:19200\n\t"
    "ds_read_b64_tr_b16 v[232:233], v11 offset:19440\n\t"
    "s_waitcnt lgkmcnt(9)\n\t"
    "v_mfma_f32_32x32x16_bf16 v[46:61], v[210:213], v[206:209], v[46:61]\n\t"
    "ds_read_b128 v[234:237], v5 offset:4096\n\t"
    "s_waitcnt lgkmcnt(8)\n\t"
    "v_mfma_f32_32x32x16_bf16 v[62:77], v[210:213], v[214:217], v[62:77]\n\t"
    "s_waitcnt lgkmcnt(3)\n\t"
    "v_cmp_ne_u32 vcc, 0, v13\n\t"
    "v_mfma_f32_32x32x16_bf16 v[78:93], v[218:221], v[206:209], v[78:93]\n\t"
    "s_getpc_b64 s[18:19]\n\t"
    "s_cbranch_vccnz .Lsp48_%=\n\t"
    "ds_read_b64_tr_b16 v[238:239], v10 offset:23040\n\t"
    "ds_read_b64_tr_b16 v[240:241], v10 offset:23280\n\t"
    "ds_read_b128 v[242:245], v6 offset:0\n\t"
    "v_mfma_f32_32x32x16_bf16 v[94:109], v[218:221], v[214:217], v[94:109]\n\t"
    "ds_read_b64_tr_b16 v[246:247], v11 offset:23040\n\t"
    "ds_read_b64_tr_b16 v[248:249], v11 offset:23280\n\t"
    "v_mfma_f32_32x32x16_bf16 v[46:61], v[226:229], v[222:225], v[46:61]\n\t"
    "ds_read_b128 v[174:177], v6 offset:4096\n\t"
    "s_waitcnt lgkmcnt(7)\n\t"
    "v_mfma_f32_32x32x16_bf16 v[62:77], v[226:229], v[230:233], v[62:77]\n\t"
    "s_waitcnt lgkmcnt(6)\n\t"
    "v_mfma_f32_32x32x16_bf16 v[78:93], v[234:237], v[222:225], v[78:93]\n\t"
    "ds_read_b64_tr_b16 v[178:179], v10 offset:26880\n\t"
    "ds_read_b64_tr_b16 v[180:181], v10 offset:27120\n\t"
    "ds_read_b128 v[182:185], v8 offset:0\n\t"
    "v_mfma_f32_32x32x16_bf16 v[94:109], v[234:237], v[230:233], v[94:109]\n\t"
    "v_add_u32 v12, 8, v12\n\t"
    "ds_read_b32 v13, v12\n\t"
    "ds_read_b64_tr_b16 v[186:187], v11 offset:26880\n\t"
    "ds_read_b64_tr_b16 v[188:189], v11 offset:27120\n\t"
    "s_waitcnt lgkmcnt(9)\n\t"
    "v_mfma_f32_32x32x16_bf16 v[46:61], v[242:245], v[238:241], v[46:61]\n\t"
    "ds_read_b128 v[190:193], v8 offset:4096\n\t"
    "s_waitcnt lgkmcnt(8)\n\t"
    "v_mfma_f32_32x32x16_bf16 v[62:77], v[242:245], v[246:249], v[62:77]\n\t"
    "s_waitcnt lgkmcnt(3)\n\t"
    "v_cmp_ne_u32 vcc, 0, v13\n\t"
    "v_mfma_f32_32x32x16_bf16 v[78:93], v[174:177], v[238:241], v[78:93]\n\t"
    "s_getpc_b64 s[18:19]\n\t"
    "s_cbranch_vccnz .Lsp48_%=\n\t"
    "ds_read_b64_tr_b16 v[194:195], v21 offset:15360\n\t"
    "ds_read_b64_tr_b16 v[196:197], v21 offset:15600\n\t"
    "ds_read_b128 v[198:201], v4 offset:16384\n\t"
    "v_mfma_f32_32x32x16_bf16 v[94:109], v[174:177], v[246:249], v[94:109]\n\t"
    "ds_read_b64_tr_b16 v[202:203], v7 offset:15360\n\t"
    "ds_read_b64_tr_b16 v[204:205], v7 offset:15600\n\t"
    "v_mfma_f32_32x32x16_bf16 v[46:61], v[182:185], v[178:181], v[46:61]\n\t"
    "ds_read_b128 v[210:213], v4 offset:20480\n\t"
    "s_waitcnt lgkmcnt(7)\n\t"
    "v_mfma_f32_32x32x16_bf16 v[62:77], v[182:185], v[186:189], v[62:77]\n\t"
    "s_waitcnt lgkmcnt(6)\n\t"
    "v_mfma_f32_32x32x16_bf16 v[78:93], v[190:193], v[178:181], v[78:93]\n\t"
    "ds_read_b64_tr_b16 v[206:207], v21 offset:19200\n\t"
    "ds_read_b64_tr_b16 v[208:209], v21 offset:19440\n\t"
    "ds_read_b128 v[214:217], v5 offset:16384\n\t"
    "s_add_u32 m0, s17, 16\n\t"
    "s_mov_b64 exec, 1\n\t"
    "buffer_load_dword v9, s[36:39], 0 offen lds\n\t"
    "s_mov_b64 exec, -1\n\t"
    "v_mfma_f32_32x32x16_bf16 v[94:109], v[190:193], v[186:189], v[94:109]\n\t"
    "v_add_u32 v12, 8, v12\n\t"
    "ds_read_b32 v13, v12\n\t"
    "ds_read_b64_tr_b16 v[218:219], v7 offset:19200\n\t"
    "ds_read_b64_tr_b16 v[220:221], v7 offset:19440\n\t"
    "s_waitcnt lgkmcnt(9)\n\t"
    "v_mfma_f32_32x32x16_bf16 v[46:61], v[198:201], v[194:197], v[46:61]\n\t"
    "ds_read_b128 v[226:229], v5 offset:20480\n\t"
    "s_waitcnt lgkmcnt(8)\n\t"
    "v_mfma_f32_32x32x16_bf16 v[62:77], v[198:201], v[202:205], v[62:77]\n\t"
    "s_waitcnt lgkmcnt(3)\n\t"
    "v_cmp_ne_u32 vcc, 0, v13\n\t"
    "v_mfma_f32_32x32x16_bf16 v[78:93], v[210:213], v[194:197], v[78:93]\n\t"
    "s_getpc_b64 s[18:19]\n\t"
    "s_cbranch_vccnz .Lsp48_%=\n\t"
    "ds_read_b64_tr_b16 v[222:223], v21 offset:23040\n\t"
    "ds_read_b64_tr_b16 v[224:225], v21 offset:23280\n\t"
    "ds_read_b128 v[230:233], v6 offset:16384\n\t"
    "v_mfma_f32_32x32x16_bf16 v[94:109], v[210:213], v[202:205], v[94:109]\n\t"
    "ds_read_b64_tr_b16 v[234:235], v7 offset:23040\n\t"
    "ds_read_b64_tr_b16 v[236:237], v7 offset:23280\n\t"
    "v_mfma_f32_32x32x16_bf16 v[46:61], v[214:217], v[206:209], v[46:61]\n\t"
    "ds_read_b128 v[242:245], v6 offset:20480\n\t"
    "s_waitcnt lgkmcnt(7)\n\t"
    "v_mfma_f32_32x32x16_bf16 v[62:77], v[214:217], v[218:221], v[62:77]\n\t"
    "s_waitcnt lgkmcnt(6)\n\t"
    "v_mfma_f32_32x32x16_bf16 v[78:93], v[226:229], v[206:209], v[78:93]\n\t"
    "ds_read_b64_tr_b16 v[238:239], v21 offset:26880\n\t"
    "ds_read_b64_tr_b16 v[240:241], v21 offset:27120\n\t"
    "ds_read_b128 v[246:249], v8 offset:16384\n\t"
    "v_mfma_f32_32x32x16_bf16 v[94:109], v[226:229], v[218:221], v[94:109]\n\t"
    "v_add_u32 v12, 8, v12\n\t"
    "ds_read_b32 v13, v12\n\t"
    "ds_read_b64_tr_b16 v[174:175], v7 offset:26880\n\t"
    "ds_read_b64_tr_b16 v[176:177], v7 offset:27120\n\t"
    "s_waitcnt lgkmcnt(9)\n\t"
    "v_mfma_f32_32x32x16_bf16 v[46:61], v[230:233], v[222:225], v[46:61]\n\t"
    "ds_read_b128 v[182:185], v8 offset:20480\n\t"
    "s_waitcnt lgkmcnt(8)\n\t"
    "v_mfma_f32_32x32x16_bf16 v[62:77], v[230:233], v[234:237], v[62:77]\n\t"
    "s_waitcnt lgkmcnt(3)\n\t"
    "v_cmp_ne_u32 vcc, 0, v13\n\t"
    "v_mfma_f32_32x32x16_bf16 v[78:93], v[242:245], v[222:225], v[78:93]\n\t"
    "s_getpc_b64 s[18:19]\n\t"
    "s_cbranch_vccnz .Lsp48_%=\n\t"
    "ds_read_b64_tr_b16 v[178:179], v21 offset:30720\n\t"
    "ds_read_b64_tr_b16 v[180:181], v21 offset:30960\n\t"
    "ds_read_b128 v[186:189], v4 offset:32768\n\t"
    "v_mfma_f32_32x32x16_bf16 v[94:109], v[242:245], v[234:237], v[94:109]\n\t"
    "ds_read_b64_tr_b16 v[190:191], v7 offset:30720\n\t"
    "ds_read_b64_tr_b16 v[192:193], v7 offset:30960\n\t"
    "v_mfma_f32_32x32x16_bf16 v[46:61], v[246:249], v[238:241], v[46:61]\n\t"
    "ds_read_b128 v[198:201], v4 offset:36864\n\t"
    "s_waitcnt lgkmcnt(7)\n\t"
    "v_mfma_f32_32x32x16_bf16 v[62:77], v[246:249], v[174:177], v[62:77]\n\t"
    "s_waitcnt lgkmcnt(6)\n\t"
    "v_mfma_f32_32x32x16_bf16 v[78:93], v[182:185], v[238:241], v[78:93]\n\t"
    "ds_read_b64_tr_b16 v[194:195], v21 offset:34560\n\t"
    "ds_read_b64_tr_b16 v[196:197], v21 offset:34800\n\t"
    "ds_read_b128 v[202:205], v5 offset:32768\n\t"
    "s_add_u32 m0, s17, 32\n\t"
    "s_mov_b64 exec, 1\n\t"
    "buffer_load_dword v9, s[36:39], 0 offen lds\n\t"
    "s_mov_b64 exec, -1\n\t"
    "v_mfma_f32_32x32x16_bf16 v[94:109], v[182:185], v[174:177], v[94:109]\n\t"
    "v_add_u32 v12, 8, v12\n\t"
    "ds_read_b32 v13, v12\n\t"
    "ds_read_b64_tr_b16 v[210:211], v7 offset:34560\n\t"
    "ds_read_b64_tr_b16 v[212:213], v7 offset:34800\n\t"
    "s_waitcnt lgkmcnt(9)\n\t"
    "v_mfma_f32_32x32x16_bf16 v[46:61], v[186:189], v[178:181], v[46:61]\n\t"
    "ds_read_b128 v[214:217], v5 offset:36864\n\t"
    "s_waitcnt lgkmcnt(8)\n\t"
    "v_mfma_f32_32x32x16_bf16 v[62:77], v[186:189], v[190:193], v[62:77]\n\t"
    "s_waitcnt lgkmcnt(3)\n\t"
    "v_cmp_ne_u32 vcc, 0, v13\n\t"
    "v_mfma_f32_32x32x16_bf16 v[78:93], v[198:201], v[178:181], v[78:93]\n\t"
    "s_getpc_b64 s[18:19]\n\t"
    "s_cbranch_vccnz .Lsp48_%=\n\t"
    "ds_read_b64_tr_b16 v[206:207], v21 offset:38400\n\t"
    "ds_read_b64_tr_b16 v[208:209], v21 offset:38640\n\t"
    "ds_read_b128 v[218:221], v6 offset:32768\n\t"
    "v_mfma_f32_32x32x16_bf16 v[94:109], v[198:201], v[190:193], v[94:109]\n\t"
    "ds_read_b64_tr_b16 v[226:227], v7 offset:38400\n\t"
    "ds_read_b64_tr_b16 v[228:229], v7 offset:38640\n\t"
    "v_mfma_f32_32x32x16_bf16 v[46:61], v[202:205], v[194:197], v[46:61]\n\t"
    "ds_read_b128 v[230:233], v6 offset:36864\n\t"
    "s_waitcnt lgkmcnt(7)\n\t"
    "v_mfma_f32_32x32x16_bf16 v[62:77], v[202:205], v[210:213], v[62:77]\n\t"
    "s_waitcnt lgkmcnt(6)\n\t"
    "v_mfma_f32_32x32x16_bf16 v[78:93], v[214:217], v[194:197], v[78:93]\n\t"
    "ds_read_b64_tr_b16 v[222:223], v21 offset:42240\n\t"
    "ds_read_b64_tr_b16 v[224:225], v21 offset:42480\n\t"
    "ds_read_b128 v[234:237], v8 offset:32768\n\t"
    "v_mfma_f32_32x32x16_bf16 v[94:109], v[214:217], v[210:213], v[94:109]\n\t"
    "v_add_u32 v12, 8, v12\n\t"
    "ds_read_b32 v13, v12\n\t"
    "ds_read_b64_tr_b16 v[242:243], v7 offset:42240\n\t"
    "ds_read_b64_tr_b16 v[244:245], v7 offset:42480\n\t"
    "s_waitcnt lgkmcnt(9)\n\t"
    "v_mfma_f32_32x32x16_bf16 v[46:61], v[218:221], v[206:209], v[46:61]\n\t"
    "ds_read_b128 v[246:249], v8 offset:36864\n\t"
    "s_waitcnt lgkmcnt(8)\n\t"
    "v_mfma_f32_32x32x16_bf16 v[62:77], v[218:221], v[226:229], v[62:77]\n\t"
    "s_waitcnt lgkmcnt(3)\n\t"
    "v_cmp_ne_u32 vcc, 0, v13\n\t"
    "v_mfma_f32_32x32x16_bf16 v[78:93], v[230:233], v[206:209], v[78:93]\n\t"
    "s_getpc_b64 s[18:19]\n\t"
    "s_cbranch_vccnz .Lsp48_%=\n\t"
    "ds_read_b64_tr_b16 v[238:239], v21 offset:46080\n\t"
    "ds_read_b64_tr_b16 v[240:241], v21 offset:46320\n\t"
    "ds_read_b128 v[174:177], v4 offset:49152\n\t"
    "v_mfma_f32_32x32x16_bf16 v[94:109], v[230:233], v[226:229], v[94:109]\n\t"
    "ds_read_b64_tr_b16 v[182:183], v7 offset:46080\n\t"
    "ds_read_b64_tr_b16 v[184:185], v7 offset:46320\n\t"
    "v_mfma_f32_32x32x16_bf16 v[46:61], v[234:237], v[222:225], v[46:61]\n\t"
    "ds_read_b128 v[186:189], v4 offset:53248\n\t"
    "s_waitcnt lgkmcnt(7)\n\t"
    "v_mfma_f32_32x32x16_bf16 v[62:77], v[234:237], v[242:245], v[62:77]\n\t"
    "s_waitcnt lgkmcnt(6)\n\t"
    "v_mfma_f32_32x32x16_bf16 v[78:93], v[246:249], v[222:225], v[78:93]\n\t"
    "ds_read_b64_tr_b16 v[178:179], v21 offset:49920\n\t"
    "ds_read_b64_tr_b16 v[180:181], v21 offset:50160\n\t"
    "ds_read_b128 v[190:193], v5 offset:49152\n\t"
    "s_add_u32 m0, s17, 48\n\t"
    "s_mov_b64 exec, 1\n\t"
    "buffer_load_dword v9, s[36:39], 0 offen lds\n\t"
    "s_mov_b64 exec, -1\n\t"
    "v_mfma_f32_32x32x16_bf16 v[94:109], v[246:249], v[242:245], v[94:109]\n\t"
    "v_add_u32 v12, 8, v12\n\t"
    "ds_read_b32 v13, v12\n\t"
    "ds_read_b64_tr_b16 v[198:199], v7 offset:49920\n\t"
    "ds_read_b64_tr_b16 v[200:201], v7 offset:50160\n\t"
    "s_waitcnt lgkmcnt(9)\n\t"
    "v_mfma_f32_32x32x16_bf16 v[46:61], v[174:177], v[238:241], v[46:61]\n\t"
    "ds_read_b128 v[202:205], v5 offset:53248\n\t"
    "s_waitcnt lgkmcnt(8)\n\t"
    "v_mfma_f32_32x32x16_bf16 v[62:77], v[174:177], v[182:185], v[62:77]\n\t"
    "s_waitcnt lgkmcnt(3)\n\t"
    "v_cmp_ne_u32 vcc, 0, v13\n\t"
    "v_mfma_f32_32x32x16_bf16 v[78:93], v[186:189], v[238:241], v[78:93]\n\t"
    "s_getpc_b64 s[18:19]\n\t"
    "s_cbranch_vccnz .Lsp48_%=\n\t"
    "ds_read_b64_tr_b16 v[194:195], v21 offset:53760\n\t"
    "ds_read_b64_tr_b16 v[196:197], v21 offset:54000\n\t"
    "ds_read_b128 v[210:213], v6 offset:49152\n\t"
    "v_mfma_f32_32x32x16_bf16 v[94:109], v[186:189], v[182:185], v[94:109]\n\t"
    "ds_read_b64_tr_b16 v[214:215], v7 offset:53760\n\t"
    "ds_read_b64_tr_b16 v[216:217], v7 offset:54000\n\t"
    "v_mfma_f32_32x32x16_bf16 v[46:61], v[190:193], v[178:181], v[46:61]\n\t"
    "ds_read_b128 v[218:221], v6 offset:53248\n\t"
    "s_waitcnt lgkmcnt(7)\n\t"
    "v_mfma_f32_32x32x16_bf16 v[62:77], v[190:193], v[198:201], v[62:77]\n\t"
    "s_waitcnt lgkmcnt(6)\n\t"
    "v_mfma_f32_32x32x16_bf16 v[78:93], v[202:205], v[178:181], v[78:93]\n\t"
    "ds_read_b64_tr_b16 v[206:207], v21 offset:57600\n\t"
    "ds_read_b64_tr_b16 v[208:209], v21 offset:57840\n\t"
    "ds_read_b128 v[226:229], v8 offset:49152\n\t"
    "v_mfma_f32_32x32x16_bf16 v[94:109], v[202:205], v[198:201], v[94:109]\n\t"
    "v_add_u32 v12, 8, v12\n\t"
    "ds_read_b32 v13, v12\n\t"
    "ds_read_b64_tr_b16 v[230:231], v7 offset:57600\n\t"
    "ds_read_b64_tr_b16 v[232:233], v7 offset:57840\n\t"
    "s_waitcnt lgkmcnt(9)\n\t"
    "v_mfma_f32_32x32x16_bf16 v[46:61], v[210:213], v[194:197], v[46:61]\n\t"
    "ds_read_b128 v[234:237], v8 offset:53248\n\t"
    "s_waitcnt lgkmcnt(8)\n\t"
    "v_mfma_f32_32x32x16_bf16 v[62:77], v[210:213], v[214:217], v[62:77]\n\t"
    "s_waitcnt lgkmcnt(3)\n\t"
    "v_cmp_ne_u32 vcc, 0, v13\n\t"
    "v_mfma_f32_32x32x16_bf16 v[78:93], v[218:221], v[194:197], v[78:93]\n\t"
    "s_getpc_b64 s[18:19]\n\t"
    "s_cbranch_vccnz .Lsp48_%=\n\t"
    "ds_read_b64_tr_b16 v[222:223], v21 offset:0\n\t"
    "ds_read_b64_tr_b16 v[224:225], v21 offset:240\n\t"
    "ds_read_b128 v[242:245], v17 offset:0\n\t"
    "v_mfma_f32_32x32x16_bf16 v[94:109], v[218:221], v[214:217], v[94:109]\n\t"
    "ds_read_b64_tr_b16 v[246:247], v7 offset:0\n\t"
    "ds_read_b64_tr_b16 v[248:249], v7 offset:240\n\t"
    "v_mfma_f32_32x32x16_bf16 v[46:61], v[226:229], v[206:209], v[46:61]\n\t"
    "ds_read_b128 v[174:177], v17 offset:4096\n\t"
    "s_waitcnt lgkmcnt(7)\n\t"
    "v_mfma_f32_32x32x16_bf16 v[62:77], v[226:229], v[230:233], v[62:77]\n\t"
    "s_waitcnt lgkmcnt(6)\n\t"
    "v_mfma_f32_32x32x16_bf16 v[78:93], v[234:237], v[206:209], v[78:93]\n\t"
    "ds_read_b64_tr_b16 v[238:239], v21 offset:3840\n\t"
    "ds_read_b64_tr_b16 v[240:241], v21 offset:4080\n\t"
    "ds_read_b128 v[182:185], v18 offset:0\n\t"
    "s_add_u32 m0, s17, 64\n\t"
    "s_mov_b64 exec, 1\n\t"
    "buffer_load_dword v9, s[36:39], 0 offen lds\n\t"
    "s_mov_b64 exec, -1\n\t"
    "v_mfma_f32_32x32x16_bf16 v[94:109], v[234:237], v[230:233], v[94:109]\n\t"
    "v_add_u32 v12, 8, v12\n\t"
    "ds_read_b32 v13, v12\n\t"
    "ds_read_b64_tr_b16 v[186:187], v7 offset:3840\n\t"
    "ds_read_b64_tr_b16 v[188:189], v7 offset:4080\n\t"
    "s_waitcnt lgkmcnt(9)\n\t"
    "v_mfma_f32_32x32x16_bf16 v[46:61], v[242:245], v[222:225], v[46:61]\n\t"
    "ds_read_b128 v[190:193], v18 offset:4096\n\t"
    "s_waitcnt lgkmcnt(8)\n\t"
    "v_mfma_f32_32x32x16_bf16 v[62:77], v[242:245], v[246:249], v[62:77]\n\t"
    "s_waitcnt lgkmcnt(3)\n\t"
    "v_cmp_ne_u32 vcc, 0, v13\n\t"
    "v_mfma_f32_32x32x16_bf16 v[78:93], v[174:177], v[222:225], v[78:93]\n\t"
    "s_getpc_b64 s[18:19]\n\t"
    "s_cbranch_vccnz .Lsp48_%=\n\t"
    "ds_read_b64_tr_b16 v[178:179], v21 offset:7680\n\t"
    "ds_read_b64_tr_b16 v[180:181], v21 offset:7920\n\t"
    "ds_read_b128 v[198:201], v19 offset:0\n\t"
    "v_mfma_f32_32x32x16_bf16 v[94:109], v[174:177], v[246:249], v[94:109]\n\t"
    "v_add_u32 v12, 8, v12\n\t"
    "ds_read_b32 v13, v12\n\t"
    "ds_read_b64_tr_b16 v[202:203], v21 offset:11520\n\t"
    "ds_read_b64_tr_b16 v[204:205], v21 offset:11760\n\t"
    "ds_read_b128 v[210:213], v20 offset:0\n\t"
    "v_mfma_f32_32x32x16_bf16 v[46:61], v[182:185], v[238:241], v[46:61]\n\t"
    "ds_read_b128 v[194:197], v19 offset:4096\n\t"
    "s_waitcnt lgkmcnt(9)\n\t"
    "v_mfma_f32_32x32x16_bf16 v[62:77], v[182:185], v[186:189], v[62:77]\n\t"
    "ds_read_b128 v[214:217], v20 offset:4096\n\t"
    "s_waitcnt lgkmcnt(5)\n\t"
    "v_cmp_ne_u32 vcc, 0, v13\n\t"
    "v_mfma_f32_32x32x16_bf16 v[78:93], v[190:193], v[238:241], v[78:93]\n\t"
    "s_getpc_b64 s[18:19]\n\t"
    "s_cbranch_vccnz .Lsp48_%=\n\t"
    "ds_read_b64_tr_b16 v[218:219], v21 offset:15360\n\t"
    "ds_read_b64_tr_b16 v[220:221], v21 offset:15600\n\t"
    "ds_read_b128 v[226:229], v17 offset:16384\n\t"
    "v_mfma_f32_32x32x16_bf16 v[94:109], v[190:193], v[186:189], v[94:109]\n\t"
    "v_add_u32 v12, 8, v12\n\t"
    "ds_read_b32 v13, v12\n\t"
    "ds_read_b64_tr_b16 v[206:207], v21 offset:19200\n\t"
    "ds_read_b64_tr_b16 v[208:209], v21 offset:19440\n\t"
    "ds_read_b128 v[230:233], v18 offset:16384\n\t"
    "v_mfma_f32_32x32x16_bf16 v[46:61], v[198:201], v[178:181], v[46:61]\n\t"
    "ds_read_b128 v[234:237], v17 offset:20480\n\t"
    "s_waitcnt lgkmcnt(10)\n\t"
    "v_mfma_f32_32x32x16_bf16 v[46:61], v[210:213], v[202:205], v[46:61]\n\t"
    "ds_read_b128 v[242:245], v18 offset:20480\n\t"
    "s_waitcnt lgkmcnt(5)\n\t"
    "v_cmp_ne_u32 vcc, 0, v13\n\t"
    "v_mfma_f32_32x32x16_bf16 v[78:93], v[194:197], v[178:181], v[78:93]\n\t"
    "s_getpc_b64 s[18:19]\n\t"
    "s_cbranch_vccnz .Lsp48_%=\n\t"
    "ds_read_b64_tr_b16 v[222:223], v21 offset:23040\n\t"
    "ds_read_b64_tr_b16 v[224:225], v21 offset:23280\n\t"
    "ds_read_b128 v[246:249], v19 offset:16384\n\t"
    "v_mfma_f32_32x32x16_bf16 v[78:93], v[214:217], v[202:205], v[78:93]\n\t"
    "ds_read_b64_tr_b16 v[174:175], v21 offset:26880\n\t"
    "ds_read_b64_tr_b16 v[176:177], v21 offset:27120\n\t"
    "ds_read_b128 v[182:185], v20 offset:16384\n\t"
    "v_mfma_f32_32x32x16_bf16 v[46:61], v[226:229], v[218:221], v[46:61]\n\t"
    "ds_read_b128 v[238:241], v19 offset:20480\n\t"
    "s_waitcnt lgkmcnt(9)\n\t"
    "v_mfma_f32_32x32x16_bf16 v[46:61], v[230:233], v[206:209], v[46:61]\n\t"
    "ds_read_b128 v[186:189], v20 offset:20480\n\t"
    "s_waitcnt lgkmcnt(9)\n\t"
    "v_mfma_f32_32x32x16_bf16 v[78:93], v[234:237], v[218:221], v[78:93]\n\t"
    "ds_read_b64_tr_b16 v[190:191], v7 offset:7680\n\t"
    "ds_read_b64_tr_b16 v[192:193], v7 offset:7920\n\t"
    "s_waitcnt lgkmcnt(10)\n\t"
    "v_mfma_f32_32x32x16_bf16 v[78:93], v[242:245], v[206:209], v[78:93]\n\t"
    "ds_read_b64_tr_b16 v[178:179], v7 offset:11520\n\t"
    "ds_read_b64_tr_b16 v[180:181], v7 offset:11760\n\t"
    "s_waitcnt lgkmcnt(9)\n\t"
    "v_mfma_f32_32x32x16_bf16 v[46:61], v[246:249], v[222:225], v[46:61]\n\t"
    "ds_read_b64_tr_b16 v[202:203], v7 offset:15360\n\t"
    "ds_read_b64_tr_b16 v[204:205], v7 offset:15600\n\t"
    "s_waitcnt lgkmcnt(8)\n\t"
    "v_mfma_f32_32x32x16_bf16 v[46:61], v[182:185], v[174:177], v[46:61]\n\t"
    "ds_read_b64_tr_b16 v[218:219], v7 offset:19200\n\t"
    "ds_read_b64_tr_b16 v[220:221], v7 offset:19440\n\t"
    "s_waitcnt lgkmcnt(9)\n\t"
    "v_mfma_f32_32x32x16_bf16 v[78:93], v[238:241], v[222:225], v[78:93]\n\t"
    "ds_read_b64_tr_b16 v[206:207], v7 offset:23040\n\t"
    "ds_read_b64_tr_b16 v[208:209], v7 offset:23280\n\t"
    "s_waitcnt lgkmcnt(10)\n\t"
    "v_mfma_f32_32x32x16_bf16 v[78:93], v[186:189], v[174:177], v[78:93]\n\t"
    "ds_read_b64_tr_b16 v[222:223], v7 offset:26880\n\t"
    "ds_read_b64_tr_b16 v[224:225], v7 offset:27120\n\t"
    "s_nop 1\n\t"
    "v_fma_f32 v22, v46, s25, -s25\n\t"
    "v_fma_f32 v23, v47, s25, -s25\n\t"
    "v_fma_f32 v24, v48, s25, -s25\n\t"
    "v_fma_f32 v25, v49, s25, -s25\n\t"
    "v_exp_f32 v22, v22 clamp\n\t"
    "s_waitcnt lgkmcnt(10)\n\t"
    "v_mfma_f32_32x32x16_bf16 v[62:77], v[198:201], v[190:193], v[62:77]\n\t"
    "v_exp_f32 v23, v23 clamp\n\t"
    "v_exp_f32 v24, v24 clamp\n\t"
    "v_exp_f32 v25, v25 clamp\n\t"
    "v_max_i32 v22, v22, v46\n\t"
    "v_max_i32 v23, v23, v47\n\t"
    "s_waitcnt lgkmcnt(8)\n\t"
    "v_mfma_f32_32x32x16_bf16 v[62:77], v[210:213], v[178:181], v[62:77]\n\t"
    "v_max_i32 v24, v24, v48\n\t"
    "v_max_i32 v25, v25, v49\n\t"
    "v_cvt_pk_bf16_f32 v46, v22, v23\n\t"
    "v_cvt_pk_bf16_f32 v47, v24, v25\n\t"
    "v_fma_f32 v26, v50, s25, -s25\n\t"
    "v_fma_f32 v27, v51, s25, -s25\n\t"
    "v_fma_f32 v28, v52, s25, -s25\n\t"
    "v_fma_f32 v29, v53, s25, -s25\n\t"
    "s_add_u32 m0, s17, 80\n\t"
    "s_mov_b64 exec, 1\n\t"
    "buffer_load_dword v9, s[36:39], 0 offen lds\n\t"
    "s_mov_b64 exec, -1\n\t"
    "s_waitcnt lgkmcnt(6)\n\t"
    "v_mfma_f32_32x32x16_bf16 v[62:77], v[226:229], v[202:205], v[62:77]\n\t"
    "v_exp_f32 v26, v26 clamp\n\t"
    "v_exp_f32 v27, v27 clamp\n\t"
    "v_exp_f32 v28, v28 clamp\n\t"
    "v_exp_f32 v29, v29 clamp\n\t"
    "s_waitcnt lgkmcnt(4)\n\t"
    "v_mfma_f32_32x32x16_bf16 v[62:77], v[230:233], v[218:221], v[62:77]\n\t"
    "v_add_u32 v12, 16, v12\n\t"
    "ds_read_b32 v13, v12\n\t"
    "v_max_i32 v26, v26, v50\n\t"
    "v_max_i32 v27, v27, v51\n\t"
    "v_max_i32 v28, v28, v52\n\t"
    "v_max_i32 v29, v29, v53\n\t"
    "v_cvt_pk_bf16_f32 v48, v26, v27\n\t"
    "v_cvt_pk_bf16_f32 v49, v28, v29\n\t"
    "s_waitcnt lgkmcnt(3)\n\t"
    "v_mfma_f32_32x32x16_bf16 v[62:77], v[246:249], v[206:209], v[62:77]\n\t"
    "v_fma_f32 v22, v54, s25, -s25\n\t"
    "v_fma_f32 v23, v55, s25, -s25\n\t"
    "v_fma_f32 v24, v56, s25, -s25\n\t"
    "v_fma_f32 v25, v57, s25, -s25\n\t"
    "v_exp_f32 v22, v22 clamp\n\t"
    "v_exp_f32 v23, v23 clamp\n\t"
    "s_waitcnt lgkmcnt(1)\n\t"
    "v_mfma_f32_32x32x16_bf16 v[62:77], v[182:185], v[222:225], v[62:77]\n\t"
    "v_exp_f32 v24, v24 clamp\n\t"
    "v_exp_f32 v25, v25 clamp\n\t"
    "v_max_i32 v22, v22, v54\n\t"
    "v_max_i32 v23, v23, v55\n\t"
    "v_max_i32 v24, v24, v56\n\t"
    "v_max_i32 v25, v25, v57\n\t"
    "v_cvt_pk_bf16_f32 v50, v22, v23\n\t"
    "v_cvt_pk_bf16_f32 v51, v24, v25\n\t"
    "v_fma_f32 v26, v58, s25, -s25\n\t"
    "v_fma_f32 v27, v59, s25, -s25\n\t"
    "v_fma_f32 v28, v60, s25, -s25\n\t"
    "v_fma_f32 v29, v61, s25, -s25\n\t"
    "v_exp_f32 v26, v26 clamp\n\t"
    "s_waitcnt lgkmcnt(0)\n\t"
    "v_cmp_ne_u32 vcc, 0, v13\n\t"
    "s_add_u32 m0, s17, 96\n\t"
    "s_mov_b64 exec, 1\n\t"
    "buffer_load_dword v9, s[36:39], 0 offen lds\n\t"
    "s_mov_b64 exec, -1\n\t"
    "v_mfma_f32_32x32x16_bf16 v[94:109], v[194:197], v[190:193], v[94:109]\n\t"
    "s_getpc_b64 s[18:19]\n\t"
    "s_cbranch_vccnz .Lsp48_%=\n\t"
    "ds_read_b64_tr_b16 v[174:175], v21 offset:30720\n\t"
    "ds_read_b64_tr_b16 v[176:177], v21 offset:30960\n\t"
    "ds_read_b128 v[198:201], v4 offset:0\n\t"
    "v_exp_f32 v27, v27 clamp\n\t"
    "v_exp_f32 v28, v28 clamp\n\t"
    "v_exp_f32 v29, v29 clamp\n\t"
    "v_max_i32 v26, v26, v58\n\t"
    "v_max_i32 v27, v27, v59\n\t"
    "v_max_i32 v28, v28, v60\n\t"
    "v_max_i32 v29, v29, v61\n\t"
    "v_mfma_f32_32x32x16_bf16 v[94:109], v[214:217], v[178:181], v[94:109]\n\t"
    "ds_read_b128 v[210:213], v4 offset:4096\n\t"
    "v_cvt_pk_bf16_f32 v52, v26, v27\n\t"
    "v_cvt_pk_bf16_f32 v53, v28, v29\n\t"
    "v_cvt_pk_bf16_f32 v62, v62, v63\n\t"
    "v_cvt_pk_bf16_f32 v63, v64, v65\n\t"
    "v_cvt_pk_bf16_f32 v64, v66, v67\n\t"
    "v_cvt_pk_bf16_f32 v65, v68, v69\n\t"
    "v_cvt_pk_bf16_f32 v66, v70, v71\n\t"
    "v_cvt_pk_bf16_f32 v67, v72, v73\n\t"
    "v_cvt_pk_bf16_f32 v68, v74, v75\n\t"
    "v_cvt_pk_bf16_f32 v69, v76, v77\n\t"
    "v_fma_f32 v22, v78, s25, -s25\n\t"
    "v_fma_f32 v23, v79, s25, -s25\n\t"
    "v_fma_f32 v24, v80, s25, -s25\n\t"
    "v_fma_f32 v25, v81, s25, -s25\n\t"
    "v_mfma_f32_32x32x16_bf16 v[94:109], v[234:237], v[202:205], v[94:109]\n\t"
    "ds_read_b64_tr_b16 v[226:227], v21 offset:34560\n\t"
    "ds_read_b64_tr_b16 v[228:229], v21 offset:34800\n\t"
    "ds_read_b128 v[230:233], v5 offset:0\n\t"
    "v_exp_f32 v22, v22 clamp\n\t"
    "v_exp_f32 v23, v23 clamp\n\t"
    "v_exp_f32 v24, v24 clamp\n\t"
    "v_exp_f32 v25, v25 clamp\n\t"
    "v_mfma_f32_32x32x16_bf16 v[30:45], v[46:49], v[62:65], 0\n\t"
    "v_max_i32 v22, v22, v78\n\t"
    "v_max_i32 v23, v23, v79\n\t"
    "v_max_i32 v24, v24, v80\n\t"
    "v_max_i32 v25, v25, v81\n\t"
    "v_cvt_pk_bf16_f32 v78, v22, v23\n\t"
    "v_cvt_pk_bf16_f32 v79, v24, v25\n\t"
    "v_fma_f32 v26, v82, s25, -s25\n\t"
    "v_fma_f32 v27, v83, s25, -s25\n\t"
    "v_mfma_f32_32x32x16_bf16 v[30:45], v[50:53], v[66:69], v[30:45]\n\t"
    "v_fma_f32 v28, v84, s25, -s25\n\t"
    "v_fma_f32 v29, v85, s25, -s25\n\t"
    "v_exp_f32 v26, v26 clamp\n\t"
    "v_exp_f32 v27, v27 clamp\n\t"
    "v_exp_f32 v28, v28 clamp\n\t"
    "v_exp_f32 v29, v29 clamp\n\t"
    "v_max_i32 v26, v26, v82\n\t"
    "v_max_i32 v27, v27, v83\n\t"
    "v_max_i32 v28, v28, v84\n\t"
    "v_max_i32 v29, v29, v85\n\t"
    "v_cvt_pk_bf16_f32 v80, v26, v27\n\t"
    "v_mfma_f32_32x32x16_bf16 v[94:109], v[242:245], v[218:221], v[94:109]\n\t"
    "ds_read_b128 v[246:249], v5 offset:4096\n\t"
    "v_cvt_pk_bf16_f32 v81, v28, v29\n\t"
    "v_mfma_f32_32x32x16_bf16 v[94:109], v[238:241], v[206:209], v[94:109]\n\t"
    "ds_read_b64_tr_b16 v[182:183], v7 offset:30720\n\t"
    "ds_read_b64_tr_b16 v[184:185], v7 offset:30960\n\t"
    "ds_read_b128 v[190:193], v6 offset:0\n\t"
    "v_mfma_f32_32x32x16_bf16 v[94:109], v[186:189], v[222:225], v[94:109]\n\t"
    "ds_read_b128 v[194:197], v6 offset:4096\n\t"
    "s_waitcnt lgkmcnt(9)\n\t"
    "v_mfma_f32_32x32x16_bf16 v[110:125], v[174:177], v[198:201], v[110:125]\n\t"
    "ds_read_b64_tr_b16 v[178:179], v7 offset:34560\n\t"
    "ds_read_b64_tr_b16 v[180:181], v7 offset:34800\n\t"
    "ds_read_b128 v[214:217], v8 offset:0\n\t"
    "s_waitcnt lgkmcnt(11)\n\t"
    "v_mfma_f32_32x32x16_bf16 v[126:141], v[174:177], v[210:213], v[126:141]\n\t"
    "ds_read_b128 v[202:205], v8 offset:4096\n\t"
    "s_nop 2\n\t"
    "v_cvt_pk_bf16_f32 v94, v94, v95\n\t"
    "v_cvt_pk_bf16_f32 v95, v96, v97\n\t"
    "v_cvt_pk_bf16_f32 v96, v98, v99\n\t"
    "v_cvt_pk_bf16_f32 v97, v100, v101\n\t"
    "v_cvt_pk_bf16_f32 v98, v102, v103\n\t"
    "v_cvt_pk_bf16_f32 v99, v104, v105\n\t"
    "v_cvt_pk_bf16_f32 v100, v106, v107\n\t"
    "v_cvt_pk_bf16_f32 v101, v108, v109\n\t"
    "v_fma_f32 v22, v86, s25, -s25\n\t"
    "v_fma_f32 v23, v87, s25, -s25\n\t"
    "v_fma_f32 v24, v88, s25, -s25\n\t"
    "v_fma_f32 v25, v89, s25, -s25\n\t"
    "v_mfma_f32_32x32x16_bf16 v[30:45], v[78:81], v[94:97], v[30:45]\n\t"
    "v_exp_f32 v22, v22 clamp\n\t"
    "v_exp_f32 v23, v23 clamp\n\t"
    "v_exp_f32 v24, v24 clamp\n\t"
    "v_exp_f32 v25, v25 clamp\n\t"
    "v_max_i32 v22, v22, v86\n\t"
    "v_max_i32 v23, v23, v87\n\t"
    "v_max_i32 v24, v24, v88\n\t"
    "v_max_i32 v25, v25, v89\n\t"
    "v_cvt_pk_bf16_f32 v82, v22, v23\n\t"
    "v_cvt_pk_bf16_f32 v83, v24, v25\n\t"
    "v_fma_f32 v26, v90, s25, -s25\n\t"
    "ds_write_b32 v252, v253 offset:112\n\t"
    "s_waitcnt lgkmcnt(10)\n\t"
    "v_mfma_f32_32x32x16_bf16 v[110:125], v[226:229], v[230:233], v[110:125]\n\t"
    "ds_read_b64_tr_b16 v[234:235], v21 offset:38400\n\t"
    "ds_read_b64_tr_b16 v[236:237], v21 offset:38640\n\t"
    "ds_read_b128 v[218:221], v4 offset:16384\n\t"
    "v_fma_f32 v27, v91, s25, -s25\n\t"
    "v_fma_f32 v28, v92, s25, -s25\n\t"
    "v_fma_f32 v29, v93, s25, -s25\n\t"
    "v_exp_f32 v26, v26 clamp\n\t"
    "v_exp_f32 v27, v27 clamp\n\t"
    "v_exp_f32 v28, v28 clamp\n\t"
    "v_exp_f32 v29, v29 clamp\n\t"
    "v_max_i32 v26, v26, v90\n\t"
    "s_waitcnt lgkmcnt(12)\n\t"
    "v_mfma_f32_32x32x16_bf16 v[126:141], v[226:229], v[246:249], v[126:141]\n\t"
    "ds_read_b128 v[242:245], v4 offset:20480\n\t"
    "v_max_i32 v27, v27, v91\n\t"
    "v_max_i32 v28, v28, v92\n\t"
    "v_max_i32 v29, v29, v93\n\t"
    "v_cvt_pk_bf16_f32 v84, v26, v27\n\t"
    "v_cvt_pk_bf16_f32 v85, v28, v29\n\t"
    "s_nop 0\n\t"
    "s_nop 0\n\t"
    "v_mfma_f32_32x32x16_bf16 v[30:45], v[82:85], v[98:101], v[30:45]\n\t"
    "s_waitcnt lgkmcnt(10)\n\t"
    "v_mfma_f32_32x32x16_bf16 v[110:125], v[182:185], v[190:193], v[110:125]\n\t"
    "ds_read_b64_tr_b16 v[206:207], v21 offset:42240\n\t"
    "ds_read_b64_tr_b16 v[208:209], v21 offset:42480\n\t"
    "ds_read_b128 v[238:241], v5 offset:16384\n\t"
    "s_waitcnt lgkmcnt(12)\n\t"
    "v_mfma_f32_32x32x16_bf16 v[126:141], v[182:185], v[194:197], v[126:141]\n\t"
    "ds_read_b128 v[222:225], v5 offset:20480\n\t"
    "s_nop 3\n\t"
    "v_mul_f32 v22, s26, v42\n\t"
    "v_mov_b32 v23, v22\n\t"
    "s_nop 1\n\t"
    "v_permlane32_swap_b32 v23, v22\n\t"
    "v_mov_b32 v23, v22\n\t"
    "v_permlane32_swap_b32 v42, v44\n\t"
    "v_permlane32_swap_b32 v43, v45\n\t"
    "v_add_f32 v30, v30, v22\n\t"
    "v_add_f32 v31, v31, v22\n\t"
    "v_add_f32 v32, v32, v22\n\t"
    "v_add_f32 v33, v33, v22\n\t"
    "v_add_f32 v34, v34, v22\n\t"
    "v_add_f32 v35, v35, v22\n\t"
    "v_add_f32 v36, v36, v22\n\t"
    "v_add_f32 v37, v37, v22\n\t"
    "s_waitcnt lgkmcnt(10)\n\t"
    "v_mfma_f32_32x32x16_bf16 v[110:125], v[178:181], v[214:217], v[110:125]\n\t"
    "ds_read_b64_tr_b16 v[186:187], v7 offset:38400\n\t"
    "ds_read_b64_tr_b16 v[188:189], v7 offset:38640\n\t"
    "ds_read_b128 v[198:201], v6 offset:16384\n\t"
    "v_cvt_pk_bf16_f32 v30, v30, v31\n\t"
    "v_cvt_pk_bf16_f32 v31, v32, v33\n\t"
    "v_cvt_pk_bf16_f32 v32, v34, v35\n\t"
    "v_cvt_pk_bf16_f32 v33, v36, 0\n\t"
    "v_or_b32 v33, v16, v33\n\t"
    "v_add_f32 v38, v38, v22\n\t"
    "v_add_f32 v39, v39, v22\n\t"
    "v_add_f32 v40, v40, v22\n\t"
    "v_add_f32 v41, v41, v22\n\t"
    "v_add_f32 v42, v42, v22\n\t"
    "v_add_f32 v43, v43, v22\n\t"
    "buffer_store_dwordx4 v[30:33], v14, s[48:51], s28 offen sc1\n\t"
    "s_waitcnt lgkmcnt(12)\n\t"
    "v_mfma_f32_32x32x16_bf16 v[126:141], v[178:181], v[202:205], v[126:141]\n\t"
    "v_add_u32 v12, 16, v12\n\t"
    "ds_read_b32 v13, v12\n\t"
    "ds_read_b128 v[174:177], v6 offset:20480\n\t"
    "v_cvt_pk_bf16_f32 v34, v37, v38\n\t"
    "v_cvt_pk_bf16_f32 v35, v39, v40\n\t"
    "v_cvt_pk_bf16_f32 v36, v41, v42\n\t"
    "v_cvt_pk_bf16_f32 v37, v43, 0\n\t"
    "v_or_b32 v37, v16, v37\n\t"
    "buffer_store_dwordx4 v[34:37], v14, s[48:51], s29 offen sc1\n\t"
    "s_waitcnt lgkmcnt(10)\n\t"
    "v_mfma_f32_32x32x16_bf16 v[110:125], v[234:237], v[218:221], v[110:125]\n\t"
    "ds_read_b64_tr_b16 v[210:211], v7 offset:42240\n\t"
    "ds_read_b64_tr_b16 v[212:213], v7 offset:42480\n\t"
    "ds_read_b128 v[230:233], v8 offset:16384\n\t"
    "s_waitcnt lgkmcnt(12)\n\t"
    "v_mfma_f32_32x32x16_bf16 v[126:141], v[234:237], v[242:245], v[126:141]\n\t"
    "ds_read_b128 v[226:229], v8 offset:20480\n\t"
    "s_waitcnt lgkmcnt(5)\n\t"
    "v_cmp_ne_u32 vcc, 0, v13\n\t"
    "ds_write_b32 v252, v253 offset:128\n\t"
    "v_mfma_f32_32x32x16_bf16 v[110:125], v[206:209], v[238:241], v[110:125]\n\t"
    "s_getpc_b64 s[18:19]\n\t"
    "s_cbranch_vccnz .Lsp48_%=\n\t"
    "ds_read_b64_tr_b16 v[246:247], v21 offset:46080\n\t"
    "ds_read_b64_tr_b16 v[248:249], v21 offset:46320\n\t"
    "ds_read_b128 v[46:49], v4 offset:32768\n\t"
    "v_mfma_f32_32x32x16_bf16 v[126:141], v[206:209], v[222:225], v[126:141]\n\t"
    "ds_read_b128 v[50:53], v4 offset:36864\n\t"
    "v_mfma_f32_32x32x16_bf16 v[110:125], v[186:189], v[198:201], v[110:125]\n\t"
    "ds_read_b64_tr_b16 v[54:55], v21 offset:49920\n\t"
    "ds_read_b64_tr_b16 v[56:57], v21 offset:50160\n\t"
    "ds_read_b128 v[58:61], v5 offset:32768\n\t"
    "s_waitcnt lgkmcnt(12)\n\t"
    "v_mfma_f32_32x32x16_bf16 v[126:141], v[186:189], v[174:177], v[126:141]\n\t"
    "ds_read_b128 v[62:65], v5 offset:36864\n\t"
    "s_waitcnt lgkmcnt(10)\n\t"
    "v_mfma_f32_32x32x16_bf16 v[110:125], v[210:213], v[230:233], v[110:125]\n\t"
    "ds_read_b64_tr_b16 v[66:67], v7 offset:46080\n\t"
    "ds_read_b64_tr_b16 v[68:69], v7 offset:46320\n\t"
    "ds_read_b128 v[70:73], v6 offset:32768\n\t"
    "s_waitcnt lgkmcnt(12)\n\t"
    "v_mfma_f32_32x32x16_bf16 v[126:141], v[210:213], v[226:229], v[126:141]\n\t"
    "ds_read_b128 v[74:77], v6 offset:36864\n\t"
    "s_waitcnt lgkmcnt(9)\n\t"
    "v_mfma_f32_32x32x16_bf16 v[110:125], v[246:249], v[46:49], v[110:125]\n\t"
    "ds_read_b64_tr_b16 v[78:79], v7 offset:49920\n\t"
    "ds_read_b64_tr_b16 v[80:81], v7 offset:50160\n\t"
    "ds_read_b128 v[82:85], v8 offset:32768\n\t"
    "s_waitcnt lgkmcnt(11)\n\t"
    "v_mfma_f32_32x32x16_bf16 v[126:141], v[246:249], v[50:53], v[126:141]\n\t"
    "ds_read_b128 v[86:89], v8 offset:36864\n\t"
    "ds_write_b32 v252, v253 offset:144\n\t"
    "s_waitcnt lgkmcnt(10)\n\t"
    "v_mfma_f32_32x32x16_bf16 v[110:125], v[54:57], v[58:61], v[110:125]\n\t"
    "ds_read_b64_tr_b16 v[90:91], v21 offset:53760\n\t"
    "ds_read_b64_tr_b16 v[92:93], v21 offset:54000\n\t"
    "ds_read_b128 v[94:97], v4 offset:49152\n\t"
    "s_waitcnt lgkmcnt(12)\n\t"
    "v_mfma_f32_32x32x16_bf16 v[126:141], v[54:57], v[62:65], v[126:141]\n\t"
    "v_add_u32 v12, 16, v12\n\t"
    "ds_read_b32 v13, v12\n\t"
    "ds_read_b64_tr_b16 v[98:99], v21 offset:57600\n\t"
    "ds_read_b64_tr_b16 v[100:101], v21 offset:57840\n\t"
    "ds_read_b128 v[102:105], v5 offset:49152\n\t"
    "s_waitcnt lgkmcnt(13)\n\t"
    "v_mfma_f32_32x32x16_bf16 v[110:125], v[66:69], v[70:73], v[110:125]\n\t"
    "ds_read_b64_tr_b16 v[106:107], v7 offset:53760\n\t"
    "ds_read_b64_tr_b16 v[108:109], v7 offset:54000\n\t"
    "ds_read_b128 v[190:193], v6 offset:49152\n\t"
    "s_waitcnt lgkmcnt(15)\n\t"
    "v_mfma_f32_32x32x16_bf16 v[126:141], v[66:69], v[74:77], v[126:141]\n\t"
    "ds_read_b64_tr_b16 v[182:183], v7 offset:57600\n\t"
    "ds_read_b64_tr_b16 v[184:185], v7 offset:57840\n\t"
    "ds_read_b128 v[194:197], v8 offset:49152\n\t"
    "s_waitcnt lgkmcnt(9)\n\t"
    "v_cmp_ne_u32 vcc, 0, v13\n\t"
    "v_mfma_f32_32x32x16_bf16 v[110:125], v[78:81], v[82:85], v[110:125]\n\t"
    "s_getpc_b64 s[18:19]\n\t"
    "s_cbranch_vccnz .Lsp48_%=\n\t"
    "ds_read_b64_tr_b16 v[214:215], v21 offset:0\n\t"
    "ds_read_b64_tr_b16 v[216:217], v21 offset:240\n\t"
    "ds_read_b128 v[178:181], v17 offset:0\n\t"
    "v_mfma_f32_32x32x16_bf16 v[126:141], v[78:81], v[86:89], v[126:141]\n\t"
    "ds_read_b64_tr_b16 v[202:203], v21 offset:3840\n\t"
    "ds_read_b64_tr_b16 v[204:205], v21 offset:4080\n\t"
    "ds_read_b128 v[218:221], v18 offset:0\n\t"
    "v_mfma_f32_32x32x16_bf16 v[110:125], v[90:93], v[94:97], v[110:125]\n\t"
    "ds_read_b64_tr_b16 v[234:235], v7 offset:0\n\t"
    "ds_read_b64_tr_b16 v[236:237], v7 offset:240\n\t"
    "ds_read_b128 v[242:245], v19 offset:0\n\t"
    "s_waitcnt lgkmcnt(15)\n\t"
    "v_mfma_f32_32x32x16_bf16 v[110:125], v[98:101], v[102:105], v[110:125]\n\t"
    "ds_read_b64_tr_b16 v[238:239], v7 offset:3840\n\t"
    "ds_read_b64_tr_b16 v[240:241], v7 offset:4080\n\t"
    "ds_read_b128 v[206:209], v20 offset:0\n\t"
    "s_waitcnt lgkmcnt(15)\n\t"
    "v_mfma_f32_32x32x16_bf16 v[110:125], v[106:109], v[190:193], v[110:125]\n\t"
    "ds_read_b64_tr_b16 v[222:223], v21 offset:7680\n\t"
    "ds_read_b64_tr_b16 v[224:225], v21 offset:7920\n\t"
    "ds_read_b128 v[198:201], v17 offset:16384\n\t"
    "s_waitcnt lgkmcnt(15)\n\t"
    "v_mfma_f32_32x32x16_bf16 v[110:125], v[182:185], v[194:197], v[110:125]\n\t"
    "v_add_u32 v12, 8, v12\n\t"
    "ds_read_b32 v13, v12\n\t"
    "ds_read_b64_tr_b16 v[186:187], v21 offset:11520\n\t"
    "ds_read_b64_tr_b16 v[188:189], v21 offset:11760\n\t"
    "ds_read_b128 v[174:177], v18 offset:16384\n\t"
    "s_waitcnt lgkmcnt(15)\n\t"
    "v_mfma_f32_32x32x16_bf16 v[110:125], v[214:217], v[178:181], v[110:125]\n\t"
    "ds_read_b64_tr_b16 v[230:231], v7 offset:7680\n\t"
    "ds_read_b64_tr_b16 v[232:233], v7 offset:7920\n\t"
    "ds_read_b128 v[210:213], v19 offset:16384\n\t"
    "s_waitcnt lgkmcnt(15)\n\t"
    "v_mfma_f32_32x32x16_bf16 v[110:125], v[202:205], v[218:221], v[110:125]\n\t"
    "ds_read_b64_tr_b16 v[226:227], v7 offset:11520\n\t"
    "ds_read_b64_tr_b16 v[228:229], v7 offset:11760\n\t"
    "ds_read_b128 v[46:49], v20 offset:16384\n\t"
    "s_waitcnt lgkmcnt(9)\n\t"
    "v_cmp_ne_u32 vcc, 0, v13\n\t"
    "v_mfma_f32_32x32x16_bf16 v[110:125], v[234:237], v[242:245], v[110:125]\n\t"
    "s_getpc_b64 s[18:19]\n\t"
    "s_cbranch_vccnz .Lsp48_%=\n\t"
    "ds_read_b64_tr_b16 v[246:247], v21 offset:15360\n\t"
    "ds_read_b64_tr_b16 v[248:249], v21 offset:15600\n\t"
    "v_mfma_f32_32x32x16_bf16 v[110:125], v[238:241], v[206:209], v[110:125]\n\t"
    "ds_read_b64_tr_b16 v[50:51], v21 offset:19200\n\t"
    "ds_read_b64_tr_b16 v[52:53], v21 offset:19440\n\t"
    "v_mfma_f32_32x32x16_bf16 v[110:125], v[222:225], v[198:201], v[110:125]\n\t"
    "ds_read_b64_tr_b16 v[58:59], v7 offset:15360\n\t"
    "ds_read_b64_tr_b16 v[60:61], v7 offset:15600\n\t"
    "s_waitcnt lgkmcnt(12)\n\t"
    "v_mfma_f32_32x32x16_bf16 v[110:125], v[186:189], v[174:177], v[110:125]\n\t"
    "ds_read_b64_tr_b16 v[54:55], v7 offset:19200\n\t"
    "ds_read_b64_tr_b16 v[56:57], v7 offset:19440\n\t"
    "s_waitcnt lgkmcnt(11)\n\t"
    "v_mfma_f32_32x32x16_bf16 v[110:125], v[230:233], v[210:213], v[110:125]\n\t"
    "ds_read_b128 v[62:65], v4 offset:53248\n\t"
    "s_waitcnt lgkmcnt(9)\n\t"
    "v_mfma_f32_32x32x16_bf16 v[110:125], v[226:229], v[46:49], v[110:125]\n\t"
    "ds_read_b128 v[70:73], v5 offset:53248\n\t"
    "s_waitcnt lgkmcnt(8)\n\t"
    "v_mfma_f32_32x32x16_bf16 v[110:125], v[246:249], v[142:145], v[110:125]\n\t"
    "ds_read_b128 v[66:69], v6 offset:53248\n\t"
    "s_waitcnt lgkmcnt(7)\n\t"
    "v_mfma_f32_32x32x16_bf16 v[110:125], v[50:53], v[150:153], v[110:125]\n\t"
    "ds_read_b128 v[74:77], v8 offset:53248\n\t"
    "ds_write_b32 v252, v253 offset:160\n\t"
    "s_waitcnt lgkmcnt(7)\n\t"
    "v_mfma_f32_32x32x16_bf16 v[110:125], v[58:61], v[158:161], v[110:125]\n\t"
    "ds_read_b128 v[82:85], v17 offset:4096\n\t"
    "s_waitcnt lgkmcnt(6)\n\t"
    "v_mfma_f32_32x32x16_bf16 v[110:125], v[54:57], v[166:169], v[110:125]\n\t"
    "ds_read_b128 v[78:81], v18 offset:4096\n\t"
    "s_waitcnt lgkmcnt(6)\n\t"
    "v_mfma_f32_32x32x16_bf16 v[126:141], v[90:93], v[62:65], v[126:141]\n\t"
    "ds_read_b128 v[86:89], v19 offset:4096\n\t"
    "s_waitcnt lgkmcnt(6)\n\t"
    "v_mfma_f32_32x32x16_bf16 v[126:141], v[98:101], v[70:73], v[126:141]\n\t"
    "ds_read_b128 v[94:97], v20 offset:4096\n\t"
    "s_nop 4\n\t"
    "v_permlane32_swap_b32 v122, v124\n\t"
    "v_permlane32_swap_b32 v123, v125\n\t"
    "v_fma_f32 v22, v110, s25, -s25\n\t"
    "v_fma_f32 v23, v111, s25, -s25\n\t"
    "v_fma_f32 v24, v112, s25, -s25\n\t"
    "s_waitcnt lgkmcnt(6)\n\t"
    "v_mfma_f32_32x32x16_bf16 v[126:141], v[106:109], v[66:69], v[126:141]\n\t"
    "ds_read_b128 v[102:105], v17 offset:20480\n\t"
    "v_fma_f32 v25, v113, s25, -s25\n\t"
    "v_exp_f32 v22, v22 clamp\n\t"
    "v_exp_f32 v23, v23 clamp\n\t"
    "s_waitcnt lgkmcnt(6)\n\t"
    "v_mfma_f32_32x32x16_bf16 v[126:141], v[182:185], v[74:77], v[126:141]\n\t"
    "ds_read_b128 v[190:193], v18 offset:20480\n\t"
    "v_exp_f32 v24, v24 clamp\n\t"
    "v_exp_f32 v25, v25 clamp\n\t"
    "v_max_i32 v22, v22, v110\n\t"
    "s_waitcnt lgkmcnt(5)\n\t"
    "v_mfma_f32_32x32x16_bf16 v[126:141], v[214:217], v[82:85], v[126:141]\n\t"
    "ds_read_b128 v[194:197], v19 offset:20480\n\t"
    "v_max_i32 v23, v23, v111\n\t"
    "v_max_i32 v24, v24, v112\n\t"
    "v_max_i32 v25, v25, v113\n\t"
    "v_add_f32 v22, s26, v22\n\t"
    "v_add_f32 v23, s26, v23\n\t"
    "s_waitcnt lgkmcnt(5)\n\t"
    "v_mfma_f32_32x32x16_bf16 v[126:141], v[202:205], v[78:81], v[126:141]\n\t"
    "ds_read_b128 v[178:181], v20 offset:20480\n\t"
    "v_add_f32 v24, s26, v24\n\t"
    "v_add_f32 v25, s26, v25\n\t"
    "v_cvt_pk_bf16_f32 v110, v22, v23\n\t"
    "v_cvt_pk_bf16_f32 v111, v24, v25\n\t"
    "v_fma_f32 v26, v114, s25, -s25\n\t"
    "ds_write_b32 v252, v253 offset:176\n\t"
    "s_waitcnt lgkmcnt(6)\n\t"
    "v_mfma_f32_32x32x16_bf16 v[126:141], v[234:237], v[86:89], v[126:141]\n\t"
    "v_fma_f32 v27, v115, s25, -s25\n\t"
    "v_fma_f32 v28, v116, s25, -s25\n\t"
    "v_fma_f32 v29, v117, s25, -s25\n\t"
    "v_exp_f32 v26, v26 clamp\n\t"
    "v_exp_f32 v27, v27 clamp\n\t"
    "s_waitcnt lgkmcnt(5)\n\t"
    "v_mfma_f32_32x32x16_bf16 v[126:141], v[238:241], v[94:97], v[126:141]\n\t"
    "v_exp_f32 v28, v28 clamp\n\t"
    "v_exp_f32 v29, v29 clamp\n\t"
    "v_max_i32 v26, v26, v114\n\t"
    "v_max_i32 v27, v27, v115\n\t"
    "s_waitcnt lgkmcnt(4)\n\t"
    "v_mfma_f32_32x32x16_bf16 v[126:141], v[222:225], v[102:105], v[126:141]\n\t"
    "v_max_i32 v28, v28, v116\n\t"
    "v_max_i32 v29, v29, v117\n\t"
    "v_add_f32 v26, s26, v26\n\t"
    "v_add_f32 v27, s26, v27\n\t"
    "v_add_f32 v28, s26, v28\n\t"
    "v_add_f32 v29, s26, v29\n\t"
    "s_waitcnt lgkmcnt(3)\n\t"
    "v_mfma_f32_32x32x16_bf16 v[126:141], v[186:189], v[190:193], v[126:141]\n\t"
    "v_cvt_pk_bf16_f32 v112, v26, v27\n\t"
    "v_cvt_pk_bf16_f32 v113, v28, 0\n\t"
    "v_or_b32 v113, v16, v113\n\t"
    "v_fma_f32 v22, v118, s25, -s25\n\t"
    "v_fma_f32 v23, v119, s25, -s25\n\t"
    "v_fma_f32 v24, v120, s25, -s25\n\t"
    "s_waitcnt lgkmcnt(2)\n\t"
    "v_mfma_f32_32x32x16_bf16 v[126:141], v[230:233], v[194:197], v[126:141]\n\t"
    "v_fma_f32 v25, v121, s25, -s25\n\t"
    "v_exp_f32 v22, v22 clamp\n\t"
    "v_exp_f32 v23, v23 clamp\n\t"
    "v_exp_f32 v24, v24 clamp\n\t"
    "s_waitcnt lgkmcnt(1)\n\t"
    "v_mfma_f32_32x32x16_bf16 v[126:141], v[226:229], v[178:181], v[126:141]\n\t"
    "v_exp_f32 v25, v25 clamp\n\t"
    "v_max_i32 v22, v22, v118\n\t"
    "v_max_i32 v23, v23, v119\n\t"
    "v_max_i32 v24, v24, v120\n\t"
    "v_max_i32 v25, v25, v121\n\t"
    "v_mfma_f32_32x32x16_bf16 v[126:141], v[246:249], v[146:149], v[126:141]\n\t"
    "v_add_f32 v22, s26, v22\n\t"
    "v_add_f32 v23, s26, v23\n\t"
    "v_add_f32 v24, s26, v24\n\t"
    "v_add_f32 v25, s26, v25\n\t"
    "v_cvt_pk_bf16_f32 v114, v29, v22\n\t"
    "v_cvt_pk_bf16_f32 v115, v23, v24\n\t"
    "v_mfma_f32_32x32x16_bf16 v[126:141], v[50:53], v[154:157], v[126:141]\n\t"
    "v_fma_f32 v26, v122, s25, -s25\n\t"
    "v_fma_f32 v27, v123, s25, -s25\n\t"
    "v_exp_f32 v26, v26 clamp\n\t"
    "v_exp_f32 v27, v27 clamp\n\t"
    "v_mfma_f32_32x32x16_bf16 v[126:141], v[58:61], v[162:165], v[126:141]\n\t"
    "v_max_i32 v26, v26, v122\n\t"
    "v_max_i32 v27, v27, v123\n\t"
    "v_add_f32 v26, s26, v26\n\t"
    "v_add_f32 v27, s26, v27\n\t"
    "v_cvt_pk_bf16_f32 v116, v25, v26\n\t"
    "v_cvt_pk_bf16_f32 v117, v27, 0\n\t"
    "v_mfma_f32_32x32x16_bf16 v[126:141], v[54:57], v[170:173], v[126:141]\n\t"
    "v_or_b32 v117, v16, v117\n\t"
    "buffer_store_dwordx4 v[110:113], v15, s[48:51], s30 offen offset:0 sc1\n\t"
    "buffer_store_dwordx4 v[114:117], v15, s[48:51], s30 offen offset:1024 sc1\n\t"
    "s_nop 8\n\t"
    "v_permlane32_swap_b32 v138, v140\n\t"
    "v_permlane32_swap_b32 v139, v141\n\t"
    "v_pk_fma_f32 v[22:23], v[126:127], s[24:25], s[24:25] op_sel:[0,1,1] neg_lo:[0,0,1] neg_hi:[0,0,1]\n\t"
    "v_pk_fma_f32 v[24:25], v[128:129], s[24:25], s[24:25] op_sel:[0,1,1] neg_lo:[0,0,1] neg_hi:[0,0,1]\n\t"
    "v_exp_f32 v22, v22 clamp\n\t"
    "v_exp_f32 v23, v23 clamp\n\t"
    "v_exp_f32 v24, v24 clamp\n\t"
    "v_exp_f32 v25, v25 clamp\n\t"
    "v_max_i32 v22, v22, v126\n\t"
    "v_max_i32 v23, v23, v127\n\t"
    "v_max_i32 v24, v24, v128\n\t"
    "v_max_i32 v25, v25, v129\n\t"
    "v_pk_add_f32 v[22:23], v[22:23], s[26:27]\n\t"
    "v_pk_add_f32 v[24:25], v[24:25], s[26:27]\n\t"
    "v_cvt_pk_bf16_f32 v126, v22, v23\n\t"
    "v_cvt_pk_bf16_f32 v127, v24, v25\n\t"
    "v_pk_fma_f32 v[26:27], v[130:131], s[24:25], s[24:25] op_sel:[0,1,1] neg_lo:[0,0,1] neg_hi:[0,0,1]\n\t"
    "v_pk_fma_f32 v[28:29], v[132:133], s[24:25], s[24:25] op_sel:[0,1,1] neg_lo:[0,0,1] neg_hi:[0,0,1]\n\t"
    "v_exp_f32 v26, v26 clamp\n\t"
    "v_exp_f32 v27, v27 clamp\n\t"
    "v_exp_f32 v28, v28 clamp\n\t"
    "v_exp_f32 v29, v29 clamp\n\t"
    "v_max_i32 v26, v26, v130\n\t"
    "v_max_i32 v27, v27, v131\n\t"
    "v_max_i32 v28, v28, v132\n\t"
    "v_max_i32 v29, v29, v133\n\t"
    "v_pk_add_f32 v[26:27], v[26:27], s[26:27]\n\t"
    "v_pk_add_f32 v[28:29], v[28:29], s[26:27]\n\t"
    "v_cvt_pk_bf16_f32 v128, v26, v27\n\t"
    "v_cvt_pk_bf16_f32 v129, v28, 0\n\t"
    "v_or_b32 v129, v16, v129\n\t"
    "v_pk_fma_f32 v[22:23], v[134:135], s[24:25], s[24:25] op_sel:[0,1,1] neg_lo:[0,0,1] neg_hi:[0,0,1]\n\t"
    "v_pk_fma_f32 v[24:25], v[136:137], s[24:25], s[24:25] op_sel:[0,1,1] neg_lo:[0,0,1] neg_hi:[0,0,1]\n\t"
    "v_exp_f32 v22, v22 clamp\n\t"
    "v_exp_f32 v23, v23 clamp\n\t"
    "v_exp_f32 v24, v24 clamp\n\t"
    "v_exp_f32 v25, v25 clamp\n\t"
    "v_max_i32 v22, v22, v134\n\t"
    "v_max_i32 v23, v23, v135\n\t"
    "v_max_i32 v24, v24, v136\n\t"
    "v_max_i32 v25, v25, v137\n\t"
    "v_pk_add_f32 v[22:23], v[22:23], s[26:27]\n\t"
    "v_pk_add_f32 v[24:25], v[24:25], s[26:27]\n\t"
    "v_cvt_pk_bf16_f32 v130, v29, v22\n\t"
    "v_cvt_pk_bf16_f32 v131, v23, v24\n\t"
    "v_pk_fma_f32 v[26:27], v[138:139], s[24:25], s[24:25] op_sel:[0,1,1] neg_lo:[0,0,1] neg_hi:[0,0,1]\n\t"
    "v_exp_f32 v26, v26 clamp\n\t"
    "v_exp_f32 v27, v27 clamp\n\t"
    "v_max_i32 v26, v26, v138\n\t"
    "v_max_i32 v27, v27, v139\n\t"
    "v_pk_add_f32 v[26:27], v[26:27], s[26:27]\n\t"
    "v_cvt_pk_bf16_f32 v132, v25, v26\n\t"
    "v_cvt_pk_bf16_f32 v133, v27, 0\n\t"
    "v_or_b32 v133, v16, v133\n\t"
    "buffer_store_dwordx4 v[126:129], v15, s[48:51], s31 offen offset:0 sc1\n\t"
    "buffer_store_dwordx4 v[130:133], v15, s[48:51], s31 offen offset:1024 sc1\n\t"
    "v_bfe_u32 v4, v1, 2, 2\n\t"
    "v_bfe_u32 v5, v1, 4, 1\n\t"
    "v_and_b32 v6, 1, v1\n\t"
    "v_bfe_u32 v7, v1, 1, 1\n\t"
    "s_mul_i32 s24, s14, 8\n\t"
    "v_lshl_add_u32 v6, v6, 1, v5\n\t"
    "s_min_u32 s24, s24, 20\n\t"
    "v_lshl_add_u32 v6, v6, 1, v7\n\t"
    "v_mul_u32_u24 v5, 0x700, v2\n\t"
    "v_add_u32 v6, s24, v6\n\t"
    "v_mul_u32_u24 v4, 0x1c0, v4\n\t"
    "v_lshl_add_u32 v5, v6, 3, v5\n\t"
    "v_and_b32 v12, 3, v1\n\t"
    "v_add_u32 v4, v4, v5\n\t"
    "v_mul_u32_u24 v6, 0x390, v3\n\t"
    "v_add_u32 v5, 122880, v4\n\t"
    "v_lshl_add_u32 v6, v2, 4, v6\n\t"
    "v_mul_u32_u24 v7, 0x700, v3\n\t"
    "v_add_u32 v6, 64512, v6\n\t"
    "v_lshl_add_u32 v7, v2, 5, v7\n\t"
    "s_lshr_b32 s28, s12, 2\n\t"
    "s_xor_b32 s29, s11, 4\n\t"
    "s_lshl1_add_u32 s28, s29, s28\n\t"
    "s_mul_i32 s28, s28, 114688\n\t"
    "s_and_b32 s29, s12, 3\n\t"
    "s_mul_i32 s29, s29, 224\n\t"
    "s_lshl3_add_u32 s28, s24, s28\n\t"
    "s_add_u32 s28, s28, s29\n\t"
    "v_lshlrev_b32 v12, 2, v12\n\t"
    "s_add_u32 s29, s28, 57344\n\t"
    "s_movk_i32 s30, -1776\n\t"
    "v_add_u32 v12, 161000, v12\n\t"
    "v_and_b32 v3, 1, v1\n\t"
    "s_mov_b32 s22, 0x55555555\n\t"
    "v_mad_i32_i24 v7, v3, s30, v7\n\t"
    "s_mov_b32 s23, s22\n\t"
    "s_cmp_eq_u32 s14, 3\n\t"
    "s_cselect_b32 exec_hi, 0, -1\n\t"
    "s_cselect_b32 s30, 0x80000000, 0\n\t"
    "v_or_b32 v7, s30, v7\n\t"
    "s_mov_b32 exec_hi, -1\n\t"
    ".Lp50_%=:\n\t"
    "ds_read_b32 v13, v12 offset:424\n\t"
    "s_add_u32 s35, s35, 1\n\t"
    "s_cmp_lt_u32 s35, 0x400000\n\t"
    "s_waitcnt lgkmcnt(0)\n\t"
    "v_cmp_ne_u32 vcc, 0, v13\n\t"
    "s_cbranch_vccz .Lo51_%=\n\t"
    "s_sleep 1\n\t"
    "s_cbranch_scc1 .Lp50_%=\n\t"
    ".Lo51_%=:\n\t"
    "ds_read_b64_tr_b16 v[64:65], v4 offset:0\n\t"
    "ds_read_b64_tr_b16 v[66:67], v4 offset:224\n\t"
    "ds_read_b64_tr_b16 v[68:69], v4 offset:3584\n\t"
    "ds_read_b64_tr_b16 v[70:71], v4 offset:3808\n\t"
    "ds_read_b64_tr_b16 v[72:73], v4 offset:7168\n\t"
    "ds_read_b64_tr_b16 v[74:75], v4 offset:7392\n\t"
    "ds_read_b64_tr_b16 v[76:77], v4 offset:10752\n\t"
    "ds_read_b64_tr_b16 v[78:79], v4 offset:10976\n\t"
    "ds_read_b64_tr_b16 v[80:81], v4 offset:14336\n\t"
    "ds_read_b64_tr_b16 v[82:83], v4 offset:14560\n\t"
    "ds_read_b64_tr_b16 v[84:85], v4 offset:17920\n\t"
    "ds_read_b64_tr_b16 v[86:87], v4 offset:18144\n\t"
    "ds_read_b64_tr_b16 v[88:89], v4 offset:21504\n\t"
    "ds_read_b64_tr_b16 v[90:91], v4 offset:21728\n\t"
    "ds_read_b64_tr_b16 v[92:93], v4 offset:25088\n\t"
    "ds_read_b64_tr_b16 v[94:95], v4 offset:25312\n\t"
    "ds_read_b64_tr_b16 v[96:97], v4 offset:28672\n\t"
    "ds_read_b64_tr_b16 v[98:99], v4 offset:28896\n\t"
    "ds_read_b64_tr_b16 v[100:101], v4 offset:32256\n\t"
    "ds_read_b64_tr_b16 v[102:103], v4 offset:32480\n\t"
    "ds_read_b64_tr_b16 v[104:105], v4 offset:35840\n\t"
    "ds_read_b64_tr_b16 v[106:107], v4 offset:36064\n\t"
    "ds_read_b64_tr_b16 v[108:109], v4 offset:39424\n\t"
    "ds_read_b64_tr_b16 v[110:111], v4 offset:39648\n\t"
    "ds_read_b64_tr_b16 v[112:113], v4 offset:43008\n\t"
    "ds_read_b64_tr_b16 v[114:115], v4 offset:43232\n\t"
    "ds_read_b64_tr_b16 v[116:117], v4 offset:46592\n\t"
    "ds_read_b64_tr_b16 v[118:119], v4 offset:46816\n\t"
    "ds_read_b64_tr_b16 v[120:121], v4 offset:50176\n\t"
    "ds_read_b64_tr_b16 v[122:123], v4 offset:50400\n\t"
    "ds_read_b64_tr_b16 v[124:125], v4 offset:53760\n\t"
    "ds_read_b64_tr_b16 v[126:127], v4 offset:53984\n\t"
    "ds_read_b64_tr_b16 v[136:137], v5 offset:0\n\t"
    "ds_read_b64_tr_b16 v[138:139], v5 offset:224\n\t"
    "ds_read_b64_tr_b16 v[140:141], v5 offset:3584\n\t"
    "ds_read_b64_tr_b16 v[142:143], v5 offset:3808\n\t"
    "ds_read_b64_tr_b16 v[144:145], v5 offset:7168\n\t"
    "ds_read_b64_tr_b16 v[146:147], v5 offset:7392\n\t"
    "ds_read_b64_tr_b16 v[148:149], v5 offset:10752\n\t"
    "ds_read_b64_tr_b16 v[150:151], v5 offset:10976\n\t"
    "ds_read_b64_tr_b16 v[152:153], v5 offset:14336\n\t"
    "ds_read_b64_tr_b16 v[154:155], v5 offset:14560\n\t"
    "ds_read_b64_tr_b16 v[156:157], v5 offset:17920\n\t"
    "ds_read_b64_tr_b16 v[158:159], v5 offset:18144\n\t"
    ".Lp52_%=:\n\t"
    "ds_read_b32 v13, v12\n\t"
    "s_add_u32 s35, s35, 1\n\t"
    "s_cmp_lt_u32 s35, 0x400000\n\t"
    "s_waitcnt lgkmcnt(0)\n\t"
    "v_cmp_ne_u32 vcc, 0, v13\n\t"
    "s_cbranch_vccz .Lo53_%=\n\t"
    "s_sleep 1\n\t"
    "s_cbranch_scc1 .Lp52_%=\n\t"
    ".Lo53_%=:\n\t"
    "ds_read_b64_tr_b16 v[128:129], v4 offset:57344\n\t"
    "ds_read_b64_tr_b16 v[130:131], v4 offset:57568\n\t"
    "ds_read_b64_tr_b16 v[132:133], v4 offset:60928\n\t"
    "ds_read_b64_tr_b16 v[134:135], v4 offset:61152\n\t"
    "ds_read_b64_tr_b16 v[160:161], v5 offset:21504\n\t"
    "ds_read_b64_tr_b16 v[162:163], v5 offset:21728\n\t"
    "ds_read_b64_tr_b16 v[164:165], v5 offset:25088\n\t"
    "ds_read_b64_tr_b16 v[166:167], v5 offset:25312\n\t"
    "ds_read_b64_tr_b16 v[168:169], v5 offset:28672\n\t"
    "ds_read_b64_tr_b16 v[170:171], v5 offset:28896\n\t"
    "ds_read_b64_tr_b16 v[172:173], v5 offset:32256\n\t"
    "ds_read_b64_tr_b16 v[174:175], v5 offset:32480\n\t"
    "v_add_u32 v12, 264, v12\n\t"
    ".Lp54_%=:\n\t"
    "ds_read_b32 v13, v12\n\t"
    "s_add_u32 s35, s35, 1\n\t"
    "s_cmp_lt_u32 s35, 0x400000\n\t"
    "s_waitcnt lgkmcnt(0)\n\t"
    "v_cmp_ne_u32 vcc, 0, v13\n\t"
    "s_cbranch_vccz .Lo55_%=\n\t"
    "s_cbranch_scc1 .Lp54_%=\n\t"
    ".Lo55_%=:\n\t"
    "s_setprio 0\n\t"
    "ds_read_b128 v[176:179], v6 offset:0\n\t"
    "ds_read_b128 v[180:183], v6 offset:32\n\t"
    "ds_read_b128 v[184:187], v6 offset:64\n\t"
    "ds_read_b128 v[188:191], v6 offset:96\n\t"
    "s_waitcnt lgkmcnt(3)\n\t"
    "v_mfma_f32_32x32x16_bf16 v[18:33], v[64:67], v[176:179], 0\n\t"
    "s_waitcnt lgkmcnt(2)\n\t"
    "v_mfma_f32_32x32x16_bf16 v[18:33], v[68:71], v[180:183], v[18:33]\n\t"
    "v_add_u32 v12, 16, v12\n\t"
    "ds_read_b32 v13, v12\n\t"
    "ds_read_b128 v[192:195], v6 offset:128\n\t"
    "s_waitcnt lgkmcnt(3)\n\t"
    "v_mfma_f32_32x32x16_bf16 v[18:33], v[72:75], v[184:187], v[18:33]\n\t"
    "ds_read_b128 v[196:199], v6 offset:160\n\t"
    "s_waitcnt lgkmcnt(3)\n\t"
    "v_mfma_f32_32x32x16_bf16 v[18:33], v[76:79], v[188:191], v[18:33]\n\t"
    "ds_read_b128 v[200:203], v6 offset:192\n\t"
    "s_waitcnt lgkmcnt(3)\n\t"
    "v_cmp_ne_u32 vcc, 0, v13\n\t"
    "s_waitcnt lgkmcnt(2)\n\t"
    "v_mfma_f32_32x32x16_bf16 v[18:33], v[80:83], v[192:195], v[18:33]\n\t"
    "s_getpc_b64 s[18:19]\n\t"
    "s_cbranch_vccnz .Lsp48_%=\n\t"
    "ds_read_b128 v[204:207], v6 offset:29184\n\t"
    "s_waitcnt lgkmcnt(2)\n\t"
    "v_mfma_f32_32x32x16_bf16 v[18:33], v[84:87], v[196:199], v[18:33]\n\t"
    "ds_read_b128 v[208:211], v6 offset:29216\n\t"
    "s_waitcnt lgkmcnt(2)\n\t"
    "v_mfma_f32_32x32x16_bf16 v[18:33], v[88:91], v[200:203], v[18:33]\n\t"
    "ds_read_b128 v[212:215], v6 offset:29248\n\t"
    "s_waitcnt lgkmcnt(2)\n\t"
    "v_mfma_f32_32x32x16_bf16 v[34:49], v[64:67], v[204:207], 0\n\t"
    "ds_read_b128 v[216:219], v6 offset:29280\n\t"
    "s_waitcnt lgkmcnt(2)\n\t"
    "v_mfma_f32_32x32x16_bf16 v[34:49], v[68:71], v[208:211], v[34:49]\n\t"
    "v_add_u32 v12, 16, v12\n\t"
    "ds_read_b32 v13, v12\n\t"
    "ds_read_b128 v[220:223], v6 offset:29312\n\t"
    "s_waitcnt lgkmcnt(3)\n\t"
    "v_mfma_f32_32x32x16_bf16 v[34:49], v[72:75], v[212:215], v[34:49]\n\t"
    "ds_read_b128 v[224:227], v6 offset:29344\n\t"
    "s_waitcnt lgkmcnt(3)\n\t"
    "v_mfma_f32_32x32x16_bf16 v[34:49], v[76:79], v[216:219], v[34:49]\n\t"
    "ds_read_b128 v[228:231], v6 offset:29376\n\t"
    "s_waitcnt lgkmcnt(3)\n\t"
    "v_cmp_ne_u32 vcc, 0, v13\n\t"
    "s_waitcnt lgkmcnt(2)\n\t"
    "v_mfma_f32_32x32x16_bf16 v[34:49], v[80:83], v[220:223], v[34:49]\n\t"
    "s_getpc_b64 s[18:19]\n\t"
    "s_cbranch_vccnz .Lsp48_%=\n\t"
    "ds_read_b128 v[232:235], v6 offset:224\n\t"
    "s_waitcnt lgkmcnt(2)\n\t"
    "v_mfma_f32_32x32x16_bf16 v[34:49], v[84:87], v[224:227], v[34:49]\n\t"
    "ds_read_b128 v[236:239], v6 offset:256\n\t"
    "s_waitcnt lgkmcnt(2)\n\t"
    "v_mfma_f32_32x32x16_bf16 v[34:49], v[88:91], v[228:231], v[34:49]\n\t"
    "ds_read_b128 v[240:243], v6 offset:288\n\t"
    "s_waitcnt lgkmcnt(2)\n\t"
    "v_mfma_f32_32x32x16_bf16 v[18:33], v[92:95], v[232:235], v[18:33]\n\t"
    "ds_read_b128 v[244:247], v6 offset:320\n\t"
    "s_waitcnt lgkmcnt(2)\n\t"
    "v_mfma_f32_32x32x16_bf16 v[18:33], v[96:99], v[236:239], v[18:33]\n\t"
    "v_add_u32 v12, 16, v12\n\t"
    "ds_read_b32 v13, v12\n\t"
    "ds_read_b128 v[248:251], v6 offset:352\n\t"
    "s_waitcnt lgkmcnt(3)\n\t"
    "v_mfma_f32_32x32x16_bf16 v[18:33], v[100:103], v[240:243], v[18:33]\n\t"
    "ds_read_b128 v[176:179], v6 offset:384\n\t"
    "s_waitcnt lgkmcnt(3)\n\t"
    "v_mfma_f32_32x32x16_bf16 v[18:33], v[104:107], v[244:247], v[18:33]\n\t"
    "ds_read_b128 v[180:183], v6 offset:416\n\t"
    "s_waitcnt lgkmcnt(3)\n\t"
    "v_cmp_ne_u32 vcc, 0, v13\n\t"
    "s_waitcnt lgkmcnt(2)\n\t"
    "v_mfma_f32_32x32x16_bf16 v[18:33], v[108:111], v[248:251], v[18:33]\n\t"
    "s_getpc_b64 s[18:19]\n\t"
    "s_cbranch_vccnz .Lsp48_%=\n\t"
    "ds_read_b128 v[184:187], v6 offset:29408\n\t"
    "s_waitcnt lgkmcnt(2)\n\t"
    "v_mfma_f32_32x32x16_bf16 v[18:33], v[112:115], v[176:179], v[18:33]\n\t"
    "ds_read_b128 v[188:191], v6 offset:29440\n\t"
    "s_waitcnt lgkmcnt(2)\n\t"
    "v_mfma_f32_32x32x16_bf16 v[18:33], v[116:119], v[180:183], v[18:33]\n\t"
    "ds_read_b128 v[192:195], v6 offset:29472\n\t"
    "s_waitcnt lgkmcnt(2)\n\t"
    "v_mfma_f32_32x32x16_bf16 v[34:49], v[92:95], v[184:187], v[34:49]\n\t"
    "ds_read_b128 v[196:199], v6 offset:29504\n\t"
    "s_waitcnt lgkmcnt(2)\n\t"
    "v_mfma_f32_32x32x16_bf16 v[34:49], v[96:99], v[188:191], v[34:49]\n\t"
    "v_add_u32 v12, 16, v12\n\t"
    "ds_read_b32 v13, v12\n\t"
    "ds_read_b128 v[200:203], v6 offset:29536\n\t"
    "s_waitcnt lgkmcnt(3)\n\t"
    "v_mfma_f32_32x32x16_bf16 v[34:49], v[100:103], v[192:195], v[34:49]\n\t"
    "ds_read_b128 v[204:207], v6 offset:29568\n\t"
    "s_waitcnt lgkmcnt(3)\n\t"
    "v_mfma_f32_32x32x16_bf16 v[34:49], v[104:107], v[196:199], v[34:49]\n\t"
    "ds_read_b128 v[208:211], v6 offset:29600\n\t"
    "s_waitcnt lgkmcnt(3)\n\t"
    "v_cmp_ne_u32 vcc, 0, v13\n\t"
    "s_waitcnt lgkmcnt(2)\n\t"
    "v_mfma_f32_32x32x16_bf16 v[34:49], v[108:111], v[200:203], v[34:49]\n\t"
    "s_getpc_b64 s[18:19]\n\t"
    "s_cbranch_vccnz .Lsp48_%=\n\t"
    "ds_read_b128 v[212:215], v6 offset:448\n\t"
    "s_waitcnt lgkmcnt(2)\n\t"
    "v_mfma_f32_32x32x16_bf16 v[34:49], v[112:115], v[204:207], v[34:49]\n\t"
    "ds_read_b128 v[216:219], v6 offset:480\n\t"
    "s_waitcnt lgkmcnt(2)\n\t"
    "v_mfma_f32_32x32x16_bf16 v[34:49], v[116:119], v[208:211], v[34:49]\n\t"
    "ds_read_b128 v[220:223], v6 offset:512\n\t"
    "s_waitcnt lgkmcnt(2)\n\t"
    "v_mfma_f32_32x32x16_bf16 v[18:33], v[120:123], v[212:215], v[18:33]\n\t"
    "ds_read_b128 v[224:227], v6 offset:544\n\t"
    "s_waitcnt lgkmcnt(2)\n\t"
    "v_mfma_f32_32x32x16_bf16 v[18:33], v[124:127], v[216:219], v[18:33]\n\t"
    "v_add_u32 v12, 16, v12\n\t"
    "ds_read_b32 v13, v12\n\t"
    "ds_read_b128 v[228:231], v6 offset:576\n\t"
    "s_waitcnt lgkmcnt(3)\n\t"
    "v_mfma_f32_32x32x16_bf16 v[18:33], v[128:131], v[220:223], v[18:33]\n\t"
    "ds_read_b128 v[232:235], v6 offset:608\n\t"
    "s_waitcnt lgkmcnt(3)\n\t"
    "v_mfma_f32_32x32x16_bf16 v[18:33], v[132:135], v[224:227], v[18:33]\n\t"
    "ds_read_b128 v[236:239], v6 offset:640\n\t"
    "s_waitcnt lgkmcnt(3)\n\t"
    "v_cmp_ne_u32 vcc, 0, v13\n\t"
    "s_waitcnt lgkmcnt(2)\n\t"
    "v_mfma_f32_32x32x16_bf16 v[18:33], v[136:139], v[228:231], v[18:33]\n\t"
    "s_getpc_b64 s[18:19]\n\t"
    "s_cbranch_vccnz .Lsp48_%=\n\t"
    "ds_read_b128 v[240:243], v6 offset:672\n\t"
    "s_waitcnt lgkmcnt(2)\n\t"
    "v_mfma_f32_32x32x16_bf16 v[18:33], v[140:143], v[232:235], v[18:33]\n\t"
    "ds_read_b128 v[244:247], v6 offset:704\n\t"
    "s_waitcnt lgkmcnt(2)\n\t"
    "v_mfma_f32_32x32x16_bf16 v[18:33], v[144:147], v[236:239], v[18:33]\n\t"
    "ds_read_b128 v[248:251], v6 offset:736\n\t"
    "s_waitcnt lgkmcnt(2)\n\t"
    "v_mfma_f32_32x32x16_bf16 v[18:33], v[148:151], v[240:243], v[18:33]\n\t"
    "ds_read_b128 v[176:179], v6 offset:768\n\t"
    "s_waitcnt lgkmcnt(2)\n\t"
    "v_mfma_f32_32x32x16_bf16 v[18:33], v[152:155], v[244:247], v[18:33]\n\t"
    "v_add_u32 v12, 16, v12\n\t"
    "ds_read_b32 v13, v12\n\t"
    "ds_read_b128 v[180:183], v6 offset:800\n\t"
    "s_waitcnt lgkmcnt(3)\n\t"
    "v_mfma_f32_32x32x16_bf16 v[18:33], v[156:159], v[248:251], v[18:33]\n\t"
    "ds_read_b128 v[184:187], v6 offset:832\n\t"
    "s_waitcnt lgkmcnt(3)\n\t"
    "v_mfma_f32_32x32x16_bf16 v[18:33], v[160:163], v[176:179], v[18:33]\n\t"
    "ds_read_b128 v[188:191], v6 offset:864\n\t"
    "s_waitcnt lgkmcnt(3)\n\t"
    "v_cmp_ne_u32 vcc, 0, v13\n\t"
    "s_waitcnt lgkmcnt(2)\n\t"
    "v_mfma_f32_32x32x16_bf16 v[18:33], v[164:167], v[180:183], v[18:33]\n\t"
    "s_getpc_b64 s[18:19]\n\t"
    "s_cbranch_vccnz .Lsp48_%=\n\t"
    "ds_read_b128 v[192:195], v6 offset:29632\n\t"
    "s_waitcnt lgkmcnt(2)\n\t"
    "v_mfma_f32_32x32x16_bf16 v[18:33], v[168:171], v[184:187], v[18:33]\n\t"
    "ds_read_b128 v[196:199], v6 offset:29664\n\t"
    "s_waitcnt lgkmcnt(2)\n\t"
    "v_mfma_f32_32x32x16_bf16 v[18:33], v[172:175], v[188:191], v[18:33]\n\t"
    "ds_read_b128 v[200:203], v6 offset:29696\n\t"
    "s_waitcnt lgkmcnt(2)\n\t"
    "v_mfma_f32_32x32x16_bf16 v[34:49], v[120:123], v[192:195], v[34:49]\n\t"
    "ds_read_b128 v[204:207], v6 offset:29728\n\t"
    "s_waitcnt lgkmcnt(2)\n\t"
    "v_mfma_f32_32x32x16_bf16 v[34:49], v[124:127], v[196:199], v[34:49]\n\t"
    "v_add_u32 v12, 16, v12\n\t"
    "ds_read_b32 v13, v12\n\t"
    "ds_read_b128 v[208:211], v6 offset:29760\n\t"
    "s_waitcnt lgkmcnt(3)\n\t"
    "v_mfma_f32_32x32x16_bf16 v[34:49], v[128:131], v[200:203], v[34:49]\n\t"
    "ds_read_b128 v[212:215], v6 offset:29792\n\t"
    "s_waitcnt lgkmcnt(3)\n\t"
    "v_mfma_f32_32x32x16_bf16 v[34:49], v[132:135], v[204:207], v[34:49]\n\t"
    "ds_read_b128 v[216:219], v6 offset:29824\n\t"
    "v_cvt_pk_bf16_f32 v18, v18, v19\n\t"
    "v_cvt_pk_bf16_f32 v19, v20, v21\n\t"
    "v_cvt_pk_bf16_f32 v20, v22, v23\n\t"
    "v_cvt_pk_bf16_f32 v21, v24, v25\n\t"
    "v_cvt_pk_bf16_f32 v22, v26, v27\n\t"
    "v_cvt_pk_bf16_f32 v23, v28, v29\n\t"
    "s_waitcnt lgkmcnt(3)\n\t"
    "v_cmp_ne_u32 vcc, 0, v13\n\t"
    "s_waitcnt lgkmcnt(2)\n\t"
    "v_mfma_f32_32x32x16_bf16 v[34:49], v[136:139], v[208:211], v[34:49]\n\t"
    "s_getpc_b64 s[18:19]\n\t"
    "s_cbranch_vccnz .Lsp48_%=\n\t"
    "ds_read_b128 v[220:223], v6 offset:29856\n\t"
    "v_cvt_pk_bf16_f32 v24, v30, v31\n\t"
    "v_cvt_pk_bf16_f32 v25, v32, v33\n\t"
    "s_mov_b64 vcc, s[22:23]\n\t"
    "v_cndmask_b32_dpp v26, v22, v18, vcc quad_perm:[0,0,2,2] row_mask:0xf bank_mask:0xf\n\t"
    "v_cndmask_b32_dpp v27, v23, v19, vcc quad_perm:[0,0,2,2] row_mask:0xf bank_mask:0xf\n\t"
    "v_cndmask_b32_dpp v28, v24, v20, vcc quad_perm:[0,0,2,2] row_mask:0xf bank_mask:0xf\n\t"
    "v_cndmask_b32_dpp v29, v25, v21, vcc quad_perm:[0,0,2,2] row_mask:0xf bank_mask:0xf\n\t"
    "s_waitcnt lgkmcnt(2)\n\t"
    "v_mfma_f32_32x32x16_bf16 v[34:49], v[140:143], v[212:215], v[34:49]\n\t"
    "ds_read_b128 v[224:227], v6 offset:29888\n\t"
    "buffer_store_dwordx4 v[26:29], v7, s[52:55], s28 offen offset:896\n\t"
    "s_not_b64 vcc, s[22:23]\n\t"
    "v_cndmask_b32_dpp v22, v18, v22, vcc quad_perm:[1,1,3,3] row_mask:0xf bank_mask:0xf\n\t"
    "v_cndmask_b32_dpp v23, v19, v23, vcc quad_perm:[1,1,3,3] row_mask:0xf bank_mask:0xf\n\t"
    "v_cndmask_b32_dpp v24, v20, v24, vcc quad_perm:[1,1,3,3] row_mask:0xf bank_mask:0xf\n\t"
    "v_cndmask_b32_dpp v25, v21, v25, vcc quad_perm:[1,1,3,3] row_mask:0xf bank_mask:0xf\n\t"
    "s_waitcnt lgkmcnt(2)\n\t"
    "v_mfma_f32_32x32x16_bf16 v[34:49], v[144:147], v[216:219], v[34:49]\n\t"
    "ds_read_b128 v[228:231], v6 offset:29920\n\t"
    "buffer_store_dwordx4 v[22:25], v7, s[52:55], s28 offen offset:2688\n\t"
    "s_waitcnt lgkmcnt(2)\n\t"
    "v_mfma_f32_32x32x16_bf16 v[34:49], v[148:151], v[220:223], v[34:49]\n\t"
    "ds_read_b128 v[232:235], v6 offset:29952\n\t"
    "s_waitcnt lgkmcnt(2)\n\t"
    "v_mfma_f32_32x32x16_bf16 v[34:49], v[152:155], v[224:227], v[34:49]\n\t"
    "ds_read_b128 v[236:239], v6 offset:29984\n\t"
    "s_waitcnt lgkmcnt(2)\n\t"
    "v_mfma_f32_32x32x16_bf16 v[34:49], v[156:159], v[228:231], v[34:49]\n\t"
    "ds_read_b128 v[240:243], v6 offset:30016\n\t"
    "s_waitcnt lgkmcnt(2)\n\t"
    "v_mfma_f32_32x32x16_bf16 v[34:49], v[160:163], v[232:235], v[34:49]\n\t"
    "ds_read_b128 v[244:247], v6 offset:30048\n\t"
    "s_waitcnt lgkmcnt(2)\n\t"
    "v_mfma_f32_32x32x16_bf16 v[34:49], v[164:167], v[236:239], v[34:49]\n\t"
    "s_waitcnt lgkmcnt(1)\n\t"
    "v_mfma_f32_32x32x16_bf16 v[34:49], v[168:171], v[240:243], v[34:49]\n\t"
    "s_waitcnt lgkmcnt(0)\n\t"
    "v_mfma_f32_32x32x16_bf16 v[34:49], v[172:175], v[244:247], v[34:49]\n\t"
    "s_nop 11\n\t"
    "v_cvt_pk_bf16_f32 v34, v34, v35\n\t"
    "v_cvt_pk_bf16_f32 v35, v36, v37\n\t"
    "v_cvt_pk_bf16_f32 v36, v38, v39\n\t"
    "v_cvt_pk_bf16_f32 v37, v40, v41\n\t"
    "v_cvt_pk_bf16_f32 v38, v42, v43\n\t"
    "v_cvt_pk_bf16_f32 v39, v44, v45\n\t"
    "v_cvt_pk_bf16_f32 v40, v46, v47\n\t"
    "v_cvt_pk_bf16_f32 v41, v48, v49\n\t"
    "s_mov_b64 vcc, s[22:23]\n\t"
    "v_cndmask_b32_dpp v42, v38, v34, vcc quad_perm:[0,0,2,2] row_mask:0xf bank_mask:0xf\n\t"
    "v_cndmask_b32_dpp v43, v39, v35, vcc quad_perm:[0,0,2,2] row_mask:0xf bank_mask:0xf\n\t"
    "v_cndmask_b32_dpp v44, v40, v36, vcc quad_perm:[0,0,2,2] row_mask:0xf bank_mask:0xf\n\t"
    "v_cndmask_b32_dpp v45, v41, v37, vcc quad_perm:[0,0,2,2] row_mask:0xf bank_mask:0xf\n\t"
    "buffer_store_dwordx4 v[42:45], v7, s[52:55], s29 offen offset:896\n\t"
    "s_not_b64 vcc, s[22:23]\n\t"
    "v_cndmask_b32_dpp v38, v34, v38, vcc quad_perm:[1,1,3,3] row_mask:0xf bank_mask:0xf\n\t"
    "v_cndmask_b32_dpp v39, v35, v39, vcc quad_perm:[1,1,3,3] row_mask:0xf bank_mask:0xf\n\t"
    "v_cndmask_b32_dpp v40, v36, v40, vcc quad_perm:[1,1,3,3] row_mask:0xf bank_mask:0xf\n\t"
    "v_cndmask_b32_dpp v41, v37, v41, vcc quad_perm:[1,1,3,3] row_mask:0xf bank_mask:0xf\n\t"
    "buffer_store_dwordx4 v[38:41], v7, s[52:55], s29 offen offset:2688\n\t"
    "s_endpgm\n\t"
    ".LA1_%=:\n\t"
    "s_sub_u32 s11, s10, 16\n\t"
    "v_lshrrev_b32 v6, 3, v1\n\t"
    "v_bfe_u32 v7, v1, 2, 1\n\t"
    "s_lshl_b32 s22, s8, 2\n\t"
    "v_and_b32 v8, 3, v1\n\t"
    "s_lshr_b32 s22, 4132712976, s22\n\t"
    "v_mad_u32_u24 v6, v6, 3, v7\n\t"
    "s_and_b32 s22, s22, 15\n\t"
    "v_mul_u32_u24 v24, 0x380, v1\n\t"
    "s_lshr_b32 s21, s9, 1\n\t"
    "v_mad_u32_u24 v6, v6, 7, v8\n\t"
    "s_lshl3_add_u32 s24, s21, s11\n\t"
    "s_lshl3_add_u32 s25, s22, s11\n\t"
    "s_mul_i32 s24, s24, 448\n\t"
    "s_mul_i32 s25, s25, 168\n\t"
    "s_add_u32 s24, s24, s22\n\t"
    "s_and_b32 s26, s9, 1\n\t"
    "s_mul_i32 s26, s26, 3\n\t"
    "v_add_lshl_u32 v25, v6, s26, 7\n\t"
    "s_sub_u32 s25, s25, 1337\n\t"
    "s_cmp_lt_u32 s22, 8\n\t"
    "s_cselect_b32 s24, s24, 0x100000\n\t"
    "s_and_b32 s12, s8, 3\n\t"
    "s_lshl4_add_u32 s13, s9, s11\n\t"
    "s_lshr_b32 s15, s8, 2\n\t"
    "s_lshl2_add_u32 s17, s13, s12\n\t"
    "s_lshl1_add_u32 s20, s17, s15\n\t"
    "s_lshl_b32 s21, s17, 1\n\t"
    "s_lshl_b32 s20, s20, 8\n\t"
    "v_mov_b32 v3, s21\n\t"
    "v_lshl_add_u32 v2, v1, 2, s20\n\t"
    "v_mov_b32 v4, 0\n\t"
    "v_lshrrev_b32 v5, 3, v1\n\t"
    "v_bfe_u32 v6, v1, 4, 1\n\t"
    "s_lshl2_add_u32 s14, s11, s12\n\t"
    "v_mul_u32_u24 v5, 0x380, v5\n\t"
    "v_xor_b32 v6, v6, v1\n\t"
    "s_mul_i32 s30, s14, 28672\n\t"
    "v_and_b32 v6, 7, v6\n\t"
    "s_add_u32 s31, s9, 1\n\t"
    "s_sub_u32 s21, s31, 7\n\t"
    "s_min_u32 s31, s31, s21\n\t"
    "s_lshl_b32 s21, s31, 7\n\t"
    "s_lshl4_add_u32 s92, s31, s11\n\t"
    "s_lshl_b32 s92, s92, 7\n\t"
    "v_lshl_add_u32 v248, v6, 4, v5\n\t"
    "s_lshl_b32 s16, s12, 12\n\t"
    "s_add_u32 s30, s30, s21\n\t"
    "s_add_u32 s16, s16, 135168\n\t"
    "v_xor_b32 v249, 32, v248\n\t"
    "v_add_u32 v249, 6144, v249\n\t"
    "v_xor_b32 v250, 64, v248\n\t"
    "v_add_u32 v250, 12288, v250\n\t"
    "v_xor_b32 v251, 96, v248\n\t"
    "v_add_u32 v251, 18432, v251\n\t"
    "s_cmp_lt_u32 s11, 8\n\t"
    "s_cselect_b64 vcc, -1, 0\n\t"
    "s_cselect_b32 s24, s24, s25\n\t"
    "v_cndmask_b32 v119, v25, v24, vcc\n\t"
    "s_waitcnt lgkmcnt(0)\n\t"
    "s_cselect_b64 s[84:85], s[36:37], s[40:41]\n\t"
    "s_cselect_b64 s[86:87], s[38:39], s[42:43]\n\t"
    "s_lshl_b32 s19, s24, 7\n\t"
    "s_bitcmp1_b32 7, s8\n\t"
    "s_cbranch_scc0 .LA1_%=t\n\t"
    "buffer_load_dword v252, v119, s[84:87], s19 offen\n\t"
    ".LA1_%=t:\n\t"
    "s_cmp_ge_u32 s9, 7\n\t"
    "s_cbranch_scc1 .LS5_%=\n\t"
    "global_load_ushort v16, v3, s[60:61]\n\t"
    "global_load_dword v10, v2, s[64:65] nt\n\t"
    "global_load_dword v11, v2, s[66:67] nt\n\t"
    "global_load_dword v12, v2, s[68:69] nt\n\t"
    "global_load_dword v13, v2, s[70:71] nt\n\t"
    "global_load_dword v14, v2, s[72:73] nt\n\t"
    "global_load_dword v15, v2, s[74:75] nt\n\t"
    "global_load_ushort v17, v3, s[62:63]\n\t"
    "s_barrier\n\t"
    "s_cmp_ge_u32 s8, 7\n\t"
    "s_cbranch_scc1 .LA1_%=v\n\t"
    "s_bitcmp1_b32 248, s8\n\t"
    "s_cselect_b32 s20, s19, 0x80000000\n\t"
    "buffer_load_dword v252, v119, s[84:87], s20 offen\n\t"
    ".LA1_%=v:\n\t"
    "s_lshl_b32 s20, s12, 3\n\t"
    "s_lshr_b32 s20, 0x1314151a, s20\n\t"
    "s_and_b32 s20, s20, 255\n\t"
    "s_sub_u32 s20, s15, s20\n\t"
    "s_lshl1_add_u32 s21, s12, 3\n\t"
    "v_lshl_add_u32 v5, v1, 1, s20\n\t"
    "v_add_u32 v6, 1, v5\n\t"
    "v_cmp_gt_u32 vcc, s21, v5\n\t"
    "v_mov_b32 v7, 0x3f80\n\t"
    "s_mul_i32 s20, s15, 1088\n\t"
    "v_cndmask_b32 v5, 0, v7, vcc\n\t"
    "v_cmp_gt_u32 vcc, s21, v6\n\t"
    "s_lshl_b32 s21, s12, 8\n\t"
    "s_add_u32 s20, s20, s21\n\t"
    "v_cndmask_b32 v6, 0, 1.0, vcc\n\t"
    "s_add_u32 m0, s20, 151552\n\t"
    "v_or_b32 v5, v5, v6\n\t"
    "ds_write_addtid_b32 v5\n\t"
    "s_mov_b32 m0, 153856\n\t"
    "s_nop 0\n\t"
    "ds_write_addtid_b32 v4\n\t"
    "v_and_b32 v3, 31, v1\n\t"
    "v_lshrrev_b32 v2, 5, v1\n\t"
    "s_mul_i32 s18, s8, 16896\n\t"
    "v_mul_u32_u24 v20, 0x210, v3\n\t"
    "v_lshl_add_u32 v20, v2, 4, v20\n\t"
    "v_add_u32 v20, s18, v20\n\t"
    "s_waitcnt lgkmcnt(0)\n\t"
    "s_barrier\n\t"
    "s_load_dwordx2 s[78:79], s[48:49], 0x0\n\t"
    "v_mov_b64 v[54:55], 0\n\t"
    "v_mov_b64 v[56:57], 0\n\t"
    "v_mov_b64 v[58:59], 0\n\t"
    "v_mov_b64 v[60:61], 0\n\t"
    "v_mov_b64 v[62:63], 0\n\t"
    "v_mov_b64 v[64:65], 0\n\t"
    "v_mov_b64 v[66:67], 0\n\t"
    "v_mov_b32 v53, 0\n\t"
    "s_cmp_lg_u32 s15, 0\n\t"
    "s_cbranch_scc1 .Lq56_%=\n\t"
    "s_mov_b32 m0, s16\n\t"
    "s_nop 0\n\t"
    "buffer_load_dwordx4 v248, s[56:59], s30 offen offset:0 nt lds\n\t"
    "buffer_load_dwordx4 v249, s[56:59], s30 offen offset:1024 nt lds\n\t"
    "buffer_load_dwordx4 v250, s[56:59], s30 offen offset:2048 nt lds\n\t"
    "buffer_load_dwordx4 v251, s[56:59], s30 offen offset:3072 nt lds\n\t"
    "s_mov_b32 m0, s18\n\t"
    "v_and_b32 v5, 15, v1\n\t"
    "v_lshrrev_b32 v6, 4, v1\n\t"
    "v_sub_u32 v7, 15, v5\n\t"
    "v_and_b32 v8, 1, v7\n\t"
    "v_and_b32 v7, 14, v7\n\t"
    "v_lshlrev_b32 v7, 1, v7\n\t"
    "v_lshl_add_u32 v68, v6, 4, v7\n\t"
    "v_lshl_add_u32 v69, v6, 3, v7\n\t"
    "v_mul_u32_u24 v70, 0x440, v8\n\t"
    "v_add_u32 v70, v70, v68\n\t"
    "v_add_u32 v118, 151552, v70\n\t"
    "v_mul_u32_u24 v71, 0x4c0, v8\n\t"
    "v_mul_u32_u24 v72, 0x340, v8\n\t"
    "v_add3_u32 v21, v71, v68, s16\n\t"
    "s_add_u32 s20, s16, 2432\n\t"
    "v_add_u32 v22, 0x400, v21\n\t"
    "v_add3_u32 v23, v72, v69, s20\n\t"
    "v_lshlrev_b32 v7, 5, v5\n\t"
    "s_add_u32 s21, s18, 11648\n\t"
    "v_lshl_add_u32 v24, v6, 4, v7\n\t"
    "v_lshl_add_u32 v25, v6, 3, v7\n\t"
    "v_add_u32 v24, s21, v24\n\t"
    "v_add_u32 v25, s18, v25\n\t"
    "v_mul_u32_u24 v30, 16768, v6\n\t"
    "v_mov_b32 v29, s16\n\t"
    "v_and_b32 v7, 3, v1\n\t"
    "v_and_b32 v8, 12, v1\n\t"
    "v_lshl_add_u32 v30, v7, 6, v30\n\t"
    "v_lshl_add_u32 v30, v8, 2, v30\n\t"
    "v_lshl_add_u32 v31, v1, 4, s16\n\t"
    "v_lshl_add_u32 v7, v3, 1, v2\n\t"
    "s_add_u32 s21, s16, 504\n\t"
    "v_lshlrev_b32 v7, 3, v7\n\t"
    "v_sub_u32 v26, s21, v7\n\t"
    "v_and_b32 v7, 1, v1\n\t"
    "v_and_b32 v8, 30, v1\n\t"
    "v_lshl_add_u32 v68, v7, 1, v2\n\t"
    "v_sub_u32 v8, s20, v8\n\t"
    "v_cmp_eq_u32 vcc, 0, v68\n\t"
    "v_lshl_add_u32 v27, v68, 7, v8\n\t"
    "v_mul_u32_u24 v69, 158, v7\n\t"
    "v_cndmask_b32 v68, 62, 64, vcc\n\t"
    "v_lshlrev_b32 v70, 6, v2\n\t"
    "v_add_u32 v27, v27, v68\n\t"
    "v_sub_u32 v69, v69, v70\n\t"
    "v_add_u32 v8, 608, v8\n\t"
    "v_add_u32 v28, v69, v8\n\t"
    "v_and_b32 v7, 3, v1\n\t"
    "v_bfe_u32 v8, v1, 2, 2\n\t"
    "v_lshl_add_u32 v68, v7, 3, v8\n\t"
    "v_lshrrev_b32 v8, 1, v8\n\t"
    "v_lshl_add_u32 v7, v7, 1, v8\n\t"
    "v_xor_b32 v7, v7, v2\n\t"
    "v_lshlrev_b32 v7, 4, v7\n\t"
    "v_lshl_add_u32 v7, v68, 7, v7\n\t"
    "v_and_b32 v8, 1, v6\n\t"
    "v_lshl_add_u32 v7, v8, 3, v7\n\t"
    "v_add_u32 v32, s16, v7\n\t"
    "v_xor_b32 v33, 32, v32\n\t"
    "v_xor_b32 v34, 64, v32\n\t"
    "v_xor_b32 v35, 0x60, v32\n\t"
    "s_mul_i32 s22, s31, 268288\n\t"
    "s_lshl_b32 s23, s14, 8\n\t"
    "s_add_u32 s22, s22, s23\n\t"
    "s_add_u32 s22, s22, 3330048\n\t"
    "v_cmp_eq_u32 vcc, 0, v1\n\t"
    "s_waitcnt vmcnt(5)\n\t"
    "v_lshlrev_b32 v17, 16, v17\n\t"
    "v_cndmask_b32 v52, 0, v17, vcc\n\t"
    "s_branch .Lk57_%=\n\t"
    ".Lq56_%=:\n\t"
    "v_mov_b32 v52, 0\n\t"
    "v_lshrrev_b32 v7, 2, v1\n\t"
    "v_mad_u32_u24 v7, v7, 21, v8\n\t"
    "v_add_lshl_u32 v22, v7, s26, 7\n\t"
    "v_lshlrev_b32 v23, 7, v1\n\t"
    "s_lshl4_add_u32 s29, s12, s11\n\t"
    "s_mul_i32 s28, s29, 43008\n\t"
    "s_lshl_b32 s29, s29, 13\n\t"
    "s_add_u32 s27, s29, 0x40000\n\t"
    "s_cmp_ge_u32 s9, 7\n\t"
    "s_cbranch_scc1 .Ls258_%=\n\t"
    "s_cmp_lt_u32 s8, 6\n\t"
    "s_cbranch_scc0 .Lq56_%=b\n\t"
    "buffer_load_dword v252, v22, s[40:43], s28 offen\n\t"
    "s_waitcnt vmcnt(2)\n\t"
    "s_branch .Lk57_%=\n\t"
    ".Lq56_%=b:\n\t"
    "s_cmp_eq_u32 s8, 7\n\t"
    "s_cbranch_scc1 .Lq56_%=c\n\t"
    "s_waitcnt vmcnt(1)\n\t"
    "s_branch .Lk57_%=\n\t"
    ".Lq56_%=c:\n\t"
    "v_mov_b32 v116, 153904\n\t"
    "v_mov_b32 v117, 1\n\t"
    "v_lshlrev_b32 v5, 4, v1\n\t"
    "s_waitcnt lgkmcnt(0)\n\t"
    "s_add_u32 s78, s78, 0x7f4a7c15\n\t"
    "s_addc_u32 s79, s79, 0x9e3779b9\n\t"
    "v_mov_b64 v[18:19], s[78:79]\n\t"
    "s_waitcnt vmcnt(0)\n\t"
    "s_mov_b64 exec, 1\n\t"
    "ds_add_rtn_u32 v117, v116, v117\n\t"
    "s_waitcnt lgkmcnt(0)\n\t"
    "v_cmp_eq_u32 vcc, 4, v117\n\t"
    "s_cbranch_vccz .Lfl60_%=\n\t"
    "s_mov_b64 exec, 255\n\t"
    "buffer_store_dwordx4 v[18:21], v5, s[48:51], s92 offen offset:2048 sc1\n\t"
    ".Lfl60_%=:\n\t"
    "s_mov_b64 exec, -1\n\t"
    ".Lk57_%=:\n\t"
    "s_getpc_b64 s[88:89]\n\t"
    ".Lk57_%=a:\n\t"
    "s_mul_i32 s93, s15, 99\n\t"
    "s_movk_i32 s21, 99\n\t"
    "s_mov_b32 s17, 0\n\t"
    "s_add_u32 s88, s88, .Lur_%=-.Lk57_%=a\n\t"
    "s_addc_u32 s89, s89, 0\n\t"
    "v_lshlrev_b32 v16, 16, v16\n\t"
    "s_mov_b32 s24, 0x3fb8aa3b\n\t"
    "s_mov_b32 s26, 0x3e22f983\n\t"
    "v_mul_f32 v6, s24, v16\n\t"
    "v_lshlrev_b32 v68, 16, v10\n\t"
    "v_fma_f32 v7, v16, s24, -v6\n\t"
    "v_exp_f32 v6, v6\n\t"
    "v_fmac_f32 v7, 0x32a57060, v16\n\t"
    "v_and_b32 v69, 0xffff0000, v10\n\t"
    "v_mul_f32 v7, 0x3f317218, v7\n\t"
    "v_lshlrev_b32 v70, 16, v11\n\t"
    "v_fmac_f32 v6, v6, v7\n\t"
    "v_and_b32 v71, 0xffff0000, v11\n\t"
    "v_cvt_pk_bf16_f32 v6, v6, v6\n\t"
    "v_lshlrev_b32 v100, 16, v12\n\t"
    "v_and_b32 v6, 0xffff0000, v6\n\t"
    "v_lshlrev_b32 v101, 16, v13\n\t"
    "v_pk_mul_f32 v[68:69], v[68:69], v[6:7] op_sel_hi:[1,0]\n\t"
    "v_pk_mul_f32 v[70:71], v[70:71], v[6:7] op_sel_hi:[1,0]\n\t"
    "v_and_b32 v102, 0xffff0000, v12\n\t"
    "v_pk_mul_f32 v[68:69], v[68:69], s[24:25] op_sel_hi:[1,0]\n\t"
    "v_pk_mul_f32 v[70:71], v[70:71], s[26:27] op_sel_hi:[1,0]\n\t"
    "s_mov_b32 s24, 0x41000000\n\t"
    "s_mov_b32 s26, 0x43800000\n\t"
    "s_mov_b32 m0, s18\n\t"
    "v_mov_b32 v98, 0x3f80\n\t"
    "v_mov_b32 v99, 0x3f80\n\t"
    "v_pk_mul_f32 v[90:91], v[70:71], s[24:25] op_sel_hi:[1,0]\n\t"
    "v_pk_mul_f32 v[88:89], v[68:69], s[24:25] op_sel_hi:[1,0]\n\t"
    "v_fract_f32_e64 v90, v90\n\t"
    "v_fract_f32_e64 v91, v91\n\t"
    "v_exp_f32 v88, v88\n\t"
    "v_exp_f32 v89, v89\n\t"
    "v_cos_f32 v80, v90\n\t"
    "v_sin_f32 v81, v90\n\t"
    "v_cos_f32 v82, v91\n\t"
    "v_sin_f32 v83, v91\n\t"
    "v_pk_mul_f32 v[80:81], v[80:81], v[88:89] op_sel_hi:[1,0]\n\t"
    "v_pk_mul_f32 v[82:83], v[82:83], v[88:89] op_sel:[0,1]\n\t"
    "v_pk_mul_f32 v[88:89], v[80:81], v[80:81] op_sel_hi:[0,1]\n\t"
    "v_pk_mul_f32 v[90:91], v[82:83], v[82:83] op_sel_hi:[0,1]\n\t"
    "v_and_b32 v103, 0xffff0000, v13\n\t"
    "v_pk_fma_f32 v[84:85], v[80:81], v[80:81], v[88:89] op_sel:[1,1,0] op_sel_hi:[1,0,1] neg_lo:[0,1,0]\n\t"
    "v_pk_fma_f32 v[86:87], v[82:83], v[82:83], v[90:91] op_sel:[1,1,0] op_sel_hi:[1,0,1] neg_lo:[0,1,0]\n\t"
    "s_cmp_ge_u32 s8, 7\n\t"
    "s_cbranch_scc0 .Lsk62_%=\n\t"
    "s_setprio 1\n\t"
    ".Lsk62_%=:\n\t"
    "v_pk_mul_f32 v[88:89], v[80:81], v[84:85] op_sel_hi:[0,1]\n\t"
    "v_pk_mul_f32 v[90:91], v[82:83], v[86:87] op_sel_hi:[0,1]\n\t"
    "ds_write_addtid_b32 v98 offset:0\n\t"
    "v_pk_mul_f32 v[92:93], v[84:85], v[84:85] op_sel_hi:[0,1]\n\t"
    "v_pk_mul_f32 v[94:95], v[86:87], v[86:87] op_sel_hi:[0,1]\n\t"
    "v_cvt_pk_bf16_f32 v96, v80, v81\n\t"
    "ds_write_addtid_b32 v99 offset:256\n\t"
    "v_cvt_pk_bf16_f32 v97, v82, v83\n\t"
    "v_pk_fma_f32 v[80:81], v[80:81], v[84:85], v[88:89] op_sel:[1,1,0] op_sel_hi:[1,0,1] neg_lo:[0,1,0]\n\t"
    "v_pk_fma_f32 v[82:83], v[82:83], v[86:87], v[90:91] op_sel:[1,1,0] op_sel_hi:[1,0,1] neg_lo:[0,1,0]\n\t"
    "ds_write_addtid_b32 v96 offset:528\n\t"
    "v_cvt_pk_bf16_f32 v98, v84, v85\n\t"
    "v_cvt_pk_bf16_f32 v99, v86, v87\n\t"
    "v_pk_fma_f32 v[76:77], v[84:85], v[84:85], v[92:93] op_sel:[1,1,0] op_sel_hi:[1,0,1] neg_lo:[0,1,0]\n\t"
    "ds_write_addtid_b32 v97 offset:784\n\t"
    "v_pk_fma_f32 v[78:79], v[86:87], v[86:87], v[94:95] op_sel:[1,1,0] op_sel_hi:[1,0,1] neg_lo:[0,1,0]\n\t"
    "v_pk_mul_f32 v[88:89], v[80:81], v[84:85] op_sel_hi:[0,1]\n\t"
    "v_pk_mul_f32 v[90:91], v[82:83], v[86:87] op_sel_hi:[0,1]\n\t"
    "ds_write_addtid_b32 v98 offset:1056\n\t"
    "v_pk_mul_f32 v[92:93], v[76:77], v[84:85] op_sel_hi:[0,1]\n\t"
    "v_pk_mul_f32 v[94:95], v[78:79], v[86:87] op_sel_hi:[0,1]\n\t"
    "v_cvt_pk_bf16_f32 v96, v80, v81\n\t"
    "ds_write_addtid_b32 v99 offset:1312\n\t"
    "v_cvt_pk_bf16_f32 v97, v82, v83\n\t"
    "v_pk_fma_f32 v[80:81], v[80:81], v[84:85], v[88:89] op_sel:[1,1,0] op_sel_hi:[1,0,1] neg_lo:[0,1,0]\n\t"
    "v_pk_fma_f32 v[82:83], v[82:83], v[86:87], v[90:91] op_sel:[1,1,0] op_sel_hi:[1,0,1] neg_lo:[0,1,0]\n\t"
    "ds_write_addtid_b32 v96 offset:1584\n\t"
    "v_cvt_pk_bf16_f32 v98, v76, v77\n\t"
    "v_cvt_pk_bf16_f32 v99, v78, v79\n\t"
    "v_pk_fma_f32 v[76:77], v[76:77], v[84:85], v[92:93] op_sel:[1,1,0] op_sel_hi:[1,0,1] neg_lo:[0,1,0]\n\t"
    "ds_write_addtid_b32 v97 offset:1840\n\t"
    "v_pk_fma_f32 v[78:79], v[78:79], v[86:87], v[94:95] op_sel:[1,1,0] op_sel_hi:[1,0,1] neg_lo:[0,1,0]\n\t"
    "v_pk_mul_f32 v[88:89], v[80:81], v[84:85] op_sel_hi:[0,1]\n\t"
    "v_pk_mul_f32 v[90:91], v[82:83], v[86:87] op_sel_hi:[0,1]\n\t"
    "ds_write_addtid_b32 v98 offset:2112\n\t"
    "v_pk_mul_f32 v[92:93], v[76:77], v[84:85] op_sel_hi:[0,1]\n\t"
    "v_pk_mul_f32 v[94:95], v[78:79], v[86:87] op_sel_hi:[0,1]\n\t"
    "v_cvt_pk_bf16_f32 v96, v80, v81\n\t"
    "ds_write_addtid_b32 v99 offset:2368\n\t"
    "v_cvt_pk_bf16_f32 v97, v82, v83\n\t"
    "v_pk_fma_f32 v[80:81], v[80:81], v[84:85], v[88:89] op_sel:[1,1,0] op_sel_hi:[1,0,1] neg_lo:[0,1,0]\n\t"
    "v_pk_fma_f32 v[82:83], v[82:83], v[86:87], v[90:91] op_sel:[1,1,0] op_sel_hi:[1,0,1] neg_lo:[0,1,0]\n\t"
    "ds_write_addtid_b32 v96 offset:2640\n\t"
    "v_cvt_pk_bf16_f32 v98, v76, v77\n\t"
    "v_cvt_pk_bf16_f32 v99, v78, v79\n\t"
    "v_pk_fma_f32 v[76:77], v[76:77], v[84:85], v[92:93] op_sel:[1,1,0] op_sel_hi:[1,0,1] neg_lo:[0,1,0]\n\t"
    "ds_write_addtid_b32 v97 offset:2896\n\t"
    "v_pk_fma_f32 v[78:79], v[78:79], v[86:87], v[94:95] op_sel:[1,1,0] op_sel_hi:[1,0,1] neg_lo:[0,1,0]\n\t"
    "s_bitcmp1_b32 0x30, s8\n\t"
    "s_cbranch_scc0 .Lsk63_%=\n\t"
    "buffer_load_dword v252, v23, s[44:47], s29 offen\n\t"
    "buffer_load_dword v252, v23, s[44:47], s27 offen\n\t"
    ".Lsk63_%=:\n\t"
    "v_pk_mul_f32 v[88:89], v[80:81], v[84:85] op_sel_hi:[0,1]\n\t"
    "v_pk_mul_f32 v[90:91], v[82:83], v[86:87] op_sel_hi:[0,1]\n\t"
    "ds_write_addtid_b32 v98 offset:3168\n\t"
    "v_pk_mul_f32 v[92:93], v[76:77], v[84:85] op_sel_hi:[0,1]\n\t"
    "v_pk_mul_f32 v[94:95], v[78:79], v[86:87] op_sel_hi:[0,1]\n\t"
    "v_cvt_pk_bf16_f32 v96, v80, v81\n\t"
    "ds_write_addtid_b32 v99 offset:3424\n\t"
    "v_cvt_pk_bf16_f32 v97, v82, v83\n\t"
    "v_pk_fma_f32 v[80:81], v[80:81], v[84:85], v[88:89] op_sel:[1,1,0] op_sel_hi:[1,0,1] neg_lo:[0,1,0]\n\t"
    "v_pk_fma_f32 v[82:83], v[82:83], v[86:87], v[90:91] op_sel:[1,1,0] op_sel_hi:[1,0,1] neg_lo:[0,1,0]\n\t"
    "ds_write_addtid_b32 v96 offset:3696\n\t"
    "v_cvt_pk_bf16_f32 v98, v76, v77\n\t"
    "v_cvt_pk_bf16_f32 v99, v78, v79\n\t"
    "v_pk_fma_f32 v[76:77], v[76:77], v[84:85], v[92:93] op_sel:[1,1,0] op_sel_hi:[1,0,1] neg_lo:[0,1,0]\n\t"
    "ds_write_addtid_b32 v97 offset:3952\n\t"
    "v_pk_fma_f32 v[78:79], v[78:79], v[86:87], v[94:95] op_sel:[1,1,0] op_sel_hi:[1,0,1] neg_lo:[0,1,0]\n\t"
    "s_getreg_b32 s20, hwreg(HW_REG_IB_STS, 0, 4)\n\t"
    "s_cmp_eq_u32 s20, s93\n\t"
    "s_cbranch_scc0 .Lus64_%=\n\t"
    "s_swappc_b64 s[90:91], s[88:89]\n\t"
    ".Lus64_%=:\n\t"
    "v_pk_mul_f32 v[88:89], v[80:81], v[84:85] op_sel_hi:[0,1]\n\t"
    "v_pk_mul_f32 v[90:91], v[82:83], v[86:87] op_sel_hi:[0,1]\n\t"
    "ds_write_addtid_b32 v98 offset:4224\n\t"
    "v_pk_mul_f32 v[92:93], v[76:77], v[84:85] op_sel_hi:[0,1]\n\t"
    "v_pk_mul_f32 v[94:95], v[78:79], v[86:87] op_sel_hi:[0,1]\n\t"
    "v_cvt_pk_bf16_f32 v96, v80, v81\n\t"
    "ds_write_addtid_b32 v99 offset:4480\n\t"
    "v_cvt_pk_bf16_f32 v97, v82, v83\n\t"
    "v_pk_fma_f32 v[80:81], v[80:81], v[84:85], v[88:89] op_sel:[1,1,0] op_sel_hi:[1,0,1] neg_lo:[0,1,0]\n\t"
    "v_pk_fma_f32 v[82:83], v[82:83], v[86:87], v[90:91] op_sel:[1,1,0] op_sel_hi:[1,0,1] neg_lo:[0,1,0]\n\t"
    "ds_write_addtid_b32 v96 offset:4752\n\t"
    "v_cvt_pk_bf16_f32 v98, v76, v77\n\t"
    "v_cvt_pk_bf16_f32 v99, v78, v79\n\t"
    "v_pk_fma_f32 v[76:77], v[76:77], v[84:85], v[92:93] op_sel:[1,1,0] op_sel_hi:[1,0,1] neg_lo:[0,1,0]\n\t"
    "ds_write_addtid_b32 v97 offset:5008\n\t"
    "v_pk_fma_f32 v[78:79], v[78:79], v[86:87], v[94:95] op_sel:[1,1,0] op_sel_hi:[1,0,1] neg_lo:[0,1,0]\n\t"
    "v_pk_mul_f32 v[88:89], v[80:81], v[84:85] op_sel_hi:[0,1]\n\t"
    "v_pk_mul_f32 v[90:91], v[82:83], v[86:87] op_sel_hi:[0,1]\n\t"
    "ds_write_addtid_b32 v98 offset:5280\n\t"
    "v_pk_mul_f32 v[92:93], v[76:77], v[84:85] op_sel_hi:[0,1]\n\t"
    "v_pk_mul_f32 v[94:95], v[78:79], v[86:87] op_sel_hi:[0,1]\n\t"
    "v_cvt_pk_bf16_f32 v96, v80, v81\n\t"
    "ds_write_addtid_b32 v99 offset:5536\n\t"
    "v_cvt_pk_bf16_f32 v97, v82, v83\n\t"
    "v_pk_fma_f32 v[80:81], v[80:81], v[84:85], v[88:89] op_sel:[1,1,0] op_sel_hi:[1,0,1] neg_lo:[0,1,0]\n\t"
    "v_pk_fma_f32 v[82:83], v[82:83], v[86:87], v[90:91] op_sel:[1,1,0] op_sel_hi:[1,0,1] neg_lo:[0,1,0]\n\t"
    "ds_write_addtid_b32 v96 offset:5808\n\t"
    "v_cvt_pk_bf16_f32 v98, v76, v77\n\t"
    "v_cvt_pk_bf16_f32 v99, v78, v79\n\t"
    "v_pk_fma_f32 v[76:77], v[76:77], v[84:85], v[92:93] op_sel:[1,1,0] op_sel_hi:[1,0,1] neg_lo:[0,1,0]\n\t"
    "ds_write_addtid_b32 v97 offset:6064\n\t"
    "v_pk_fma_f32 v[78:79], v[78:79], v[86:87], v[94:95] op_sel:[1,1,0] op_sel_hi:[1,0,1] neg_lo:[0,1,0]\n\t"
    "s_bitcmp1_b32 112, s8\n\t"
    "s_cbranch_scc0 .Lsk65_%=\n\t"
    "s_sleep 16\n\t"
    ".Lsk65_%=:\n\t"
    "s_getreg_b32 s20, hwreg(HW_REG_IB_STS, 0, 4)\n\t"
    "s_cmp_eq_u32 s20, s93\n\t"
    "s_cbranch_scc0 .Lus66_%=\n\t"
    "s_swappc_b64 s[90:91], s[88:89]\n\t"
    ".Lus66_%=:\n\t"
    "v_pk_mul_f32 v[88:89], v[80:81], v[84:85] op_sel_hi:[0,1]\n\t"
    "v_pk_mul_f32 v[90:91], v[82:83], v[86:87] op_sel_hi:[0,1]\n\t"
    "ds_write_addtid_b32 v98 offset:6336\n\t"
    "v_pk_mul_f32 v[92:93], v[76:77], v[84:85] op_sel_hi:[0,1]\n\t"
    "v_pk_mul_f32 v[94:95], v[78:79], v[86:87] op_sel_hi:[0,1]\n\t"
    "v_cvt_pk_bf16_f32 v96, v80, v81\n\t"
    "ds_write_addtid_b32 v99 offset:6592\n\t"
    "v_cvt_pk_bf16_f32 v97, v82, v83\n\t"
    "v_pk_fma_f32 v[80:81], v[80:81], v[84:85], v[88:89] op_sel:[1,1,0] op_sel_hi:[1,0,1] neg_lo:[0,1,0]\n\t"
    "v_pk_fma_f32 v[82:83], v[82:83], v[86:87], v[90:91] op_sel:[1,1,0] op_sel_hi:[1,0,1] neg_lo:[0,1,0]\n\t"
    "ds_write_addtid_b32 v96 offset:6864\n\t"
    "v_cvt_pk_bf16_f32 v98, v76, v77\n\t"
    "v_cvt_pk_bf16_f32 v99, v78, v79\n\t"
    "v_pk_fma_f32 v[76:77], v[76:77], v[84:85], v[92:93] op_sel:[1,1,0] op_sel_hi:[1,0,1] neg_lo:[0,1,0]\n\t"
    "ds_write_addtid_b32 v97 offset:7120\n\t"
    "v_pk_fma_f32 v[78:79], v[78:79], v[86:87], v[94:95] op_sel:[1,1,0] op_sel_hi:[1,0,1] neg_lo:[0,1,0]\n\t"
    "v_pk_mul_f32 v[88:89], v[80:81], v[84:85] op_sel_hi:[0,1]\n\t"
    "v_pk_mul_f32 v[90:91], v[82:83], v[86:87] op_sel_hi:[0,1]\n\t"
    "ds_write_addtid_b32 v98 offset:7392\n\t"
    "v_pk_mul_f32 v[92:93], v[76:77], v[84:85] op_sel_hi:[0,1]\n\t"
    "v_pk_mul_f32 v[94:95], v[78:79], v[86:87] op_sel_hi:[0,1]\n\t"
    "v_cvt_pk_bf16_f32 v96, v80, v81\n\t"
    "ds_write_addtid_b32 v99 offset:7648\n\t"
    "v_cvt_pk_bf16_f32 v97, v82, v83\n\t"
    "v_pk_fma_f32 v[80:81], v[80:81], v[84:85], v[88:89] op_sel:[1,1,0] op_sel_hi:[1,0,1] neg_lo:[0,1,0]\n\t"
    "v_pk_fma_f32 v[82:83], v[82:83], v[86:87], v[90:91] op_sel:[1,1,0] op_sel_hi:[1,0,1] neg_lo:[0,1,0]\n\t"
    "ds_write_addtid_b32 v96 offset:7920\n\t"
    "v_cvt_pk_bf16_f32 v98, v76, v77\n\t"
    "v_cvt_pk_bf16_f32 v99, v78, v79\n\t"
    "v_pk_fma_f32 v[76:77], v[76:77], v[84:85], v[92:93] op_sel:[1,1,0] op_sel_hi:[1,0,1] neg_lo:[0,1,0]\n\t"
    "ds_write_addtid_b32 v97 offset:8176\n\t"
    "v_pk_fma_f32 v[78:79], v[78:79], v[86:87], v[94:95] op_sel:[1,1,0] op_sel_hi:[1,0,1] neg_lo:[0,1,0]\n\t"
    "s_getreg_b32 s20, hwreg(HW_REG_IB_STS, 0, 4)\n\t"
    "s_cmp_eq_u32 s20, s93\n\t"
    "s_cbranch_scc0 .Lus67_%=\n\t"
    "s_swappc_b64 s[90:91], s[88:89]\n\t"
    ".Lus67_%=:\n\t"
    "v_pk_mul_f32 v[88:89], v[80:81], v[84:85] op_sel_hi:[0,1]\n\t"
    "v_pk_mul_f32 v[90:91], v[82:83], v[86:87] op_sel_hi:[0,1]\n\t"
    "ds_write_addtid_b32 v98 offset:8448\n\t"
    "v_pk_mul_f32 v[92:93], v[76:77], v[84:85] op_sel_hi:[0,1]\n\t"
    "v_pk_mul_f32 v[94:95], v[78:79], v[86:87] op_sel_hi:[0,1]\n\t"
    "v_cvt_pk_bf16_f32 v96, v80, v81\n\t"
    "ds_write_addtid_b32 v99 offset:8704\n\t"
    "v_cvt_pk_bf16_f32 v97, v82, v83\n\t"
    "v_pk_fma_f32 v[80:81], v[80:81], v[84:85], v[88:89] op_sel:[1,1,0] op_sel_hi:[1,0,1] neg_lo:[0,1,0]\n\t"
    "v_pk_fma_f32 v[82:83], v[82:83], v[86:87], v[90:91] op_sel:[1,1,0] op_sel_hi:[1,0,1] neg_lo:[0,1,0]\n\t"
    "ds_write_addtid_b32 v96 offset:8976\n\t"
    "v_cvt_pk_bf16_f32 v98, v76, v77\n\t"
    "v_cvt_pk_bf16_f32 v99, v78, v79\n\t"
    "v_pk_fma_f32 v[76:77], v[76:77], v[84:85], v[92:93] op_sel:[1,1,0] op_sel_hi:[1,0,1] neg_lo:[0,1,0]\n\t"
    "ds_write_addtid_b32 v97 offset:9232\n\t"
    "v_pk_fma_f32 v[78:79], v[78:79], v[86:87], v[94:95] op_sel:[1,1,0] op_sel_hi:[1,0,1] neg_lo:[0,1,0]\n\t"
    "v_pk_mul_f32 v[88:89], v[80:81], v[84:85] op_sel_hi:[0,1]\n\t"
    "v_pk_mul_f32 v[90:91], v[82:83], v[86:87] op_sel_hi:[0,1]\n\t"
    "ds_write_addtid_b32 v98 offset:9504\n\t"
    "v_pk_mul_f32 v[92:93], v[76:77], v[84:85] op_sel_hi:[0,1]\n\t"
    "v_pk_mul_f32 v[94:95], v[78:79], v[86:87] op_sel_hi:[0,1]\n\t"
    "v_cvt_pk_bf16_f32 v96, v80, v81\n\t"
    "ds_write_addtid_b32 v99 offset:9760\n\t"
    "v_cvt_pk_bf16_f32 v97, v82, v83\n\t"
    "v_pk_fma_f32 v[80:81], v[80:81], v[84:85], v[88:89] op_sel:[1,1,0] op_sel_hi:[1,0,1] neg_lo:[0,1,0]\n\t"
    "v_pk_fma_f32 v[82:83], v[82:83], v[86:87], v[90:91] op_sel:[1,1,0] op_sel_hi:[1,0,1] neg_lo:[0,1,0]\n\t"
    "ds_write_addtid_b32 v96 offset:10032\n\t"
    "v_cvt_pk_bf16_f32 v98, v76, v77\n\t"
    "v_cvt_pk_bf16_f32 v99, v78, v79\n\t"
    "v_pk_fma_f32 v[76:77], v[76:77], v[84:85], v[92:93] op_sel:[1,1,0] op_sel_hi:[1,0,1] neg_lo:[0,1,0]\n\t"
    "ds_write_addtid_b32 v97 offset:10288\n\t"
    "v_pk_fma_f32 v[78:79], v[78:79], v[86:87], v[94:95] op_sel:[1,1,0] op_sel_hi:[1,0,1] neg_lo:[0,1,0]\n\t"
    "s_getreg_b32 s20, hwreg(HW_REG_IB_STS, 0, 4)\n\t"
    "s_cmp_eq_u32 s20, s93\n\t"
    "s_cbranch_scc0 .Lus68_%=\n\t"
    "s_swappc_b64 s[90:91], s[88:89]\n\t"
    ".Lus68_%=:\n\t"
    "v_pk_mul_f32 v[88:89], v[80:81], v[84:85] op_sel_hi:[0,1]\n\t"
    "v_pk_mul_f32 v[90:91], v[82:83], v[86:87] op_sel_hi:[0,1]\n\t"
    "ds_write_addtid_b32 v98 offset:10560\n\t"
    "v_pk_mul_f32 v[92:93], v[76:77], v[84:85] op_sel_hi:[0,1]\n\t"
    "v_pk_mul_f32 v[94:95], v[78:79], v[86:87] op_sel_hi:[0,1]\n\t"
    "v_cvt_pk_bf16_f32 v96, v80, v81\n\t"
    "ds_write_addtid_b32 v99 offset:10816\n\t"
    "v_cvt_pk_bf16_f32 v97, v82, v83\n\t"
    "v_pk_fma_f32 v[80:81], v[80:81], v[84:85], v[88:89] op_sel:[1,1,0] op_sel_hi:[1,0,1] neg_lo:[0,1,0]\n\t"
    "v_pk_fma_f32 v[82:83], v[82:83], v[86:87], v[90:91] op_sel:[1,1,0] op_sel_hi:[1,0,1] neg_lo:[0,1,0]\n\t"
    "ds_write_addtid_b32 v96 offset:11088\n\t"
    "v_cvt_pk_bf16_f32 v98, v76, v77\n\t"
    "v_cvt_pk_bf16_f32 v99, v78, v79\n\t"
    "v_pk_fma_f32 v[76:77], v[76:77], v[84:85], v[92:93] op_sel:[1,1,0] op_sel_hi:[1,0,1] neg_lo:[0,1,0]\n\t"
    "ds_write_addtid_b32 v97 offset:11344\n\t"
    "v_pk_fma_f32 v[78:79], v[78:79], v[86:87], v[94:95] op_sel:[1,1,0] op_sel_hi:[1,0,1] neg_lo:[0,1,0]\n\t"
    "v_pk_mul_f32 v[88:89], v[80:81], v[84:85] op_sel_hi:[0,1]\n\t"
    "v_pk_mul_f32 v[90:91], v[82:83], v[86:87] op_sel_hi:[0,1]\n\t"
    "ds_write_addtid_b32 v98 offset:11616\n\t"
    "v_pk_mul_f32 v[92:93], v[76:77], v[84:85] op_sel_hi:[0,1]\n\t"
    "v_pk_mul_f32 v[94:95], v[78:79], v[86:87] op_sel_hi:[0,1]\n\t"
    "v_cvt_pk_bf16_f32 v96, v80, v81\n\t"
    "ds_write_addtid_b32 v99 offset:11872\n\t"
    "v_cvt_pk_bf16_f32 v97, v82, v83\n\t"
    "v_pk_fma_f32 v[80:81], v[80:81], v[84:85], v[88:89] op_sel:[1,1,0] op_sel_hi:[1,0,1] neg_lo:[0,1,0]\n\t"
    "v_pk_fma_f32 v[82:83], v[82:83], v[86:87], v[90:91] op_sel:[1,1,0] op_sel_hi:[1,0,1] neg_lo:[0,1,0]\n\t"
    "ds_write_addtid_b32 v96 offset:12144\n\t"
    "v_cvt_pk_bf16_f32 v98, v76, v77\n\t"
    "v_cvt_pk_bf16_f32 v99, v78, v79\n\t"
    "v_pk_fma_f32 v[76:77], v[76:77], v[84:85], v[92:93] op_sel:[1,1,0] op_sel_hi:[1,0,1] neg_lo:[0,1,0]\n\t"
    "ds_write_addtid_b32 v97 offset:12400\n\t"
    "v_pk_fma_f32 v[78:79], v[78:79], v[86:87], v[94:95] op_sel:[1,1,0] op_sel_hi:[1,0,1] neg_lo:[0,1,0]\n\t"
    "s_getreg_b32 s20, hwreg(HW_REG_IB_STS, 0, 4)\n\t"
    "s_cmp_eq_u32 s20, s93\n\t"
    "s_cbranch_scc0 .Lus69_%=\n\t"
    "s_swappc_b64 s[90:91], s[88:89]\n\t"
    ".Lus69_%=:\n\t"
    "v_pk_mul_f32 v[88:89], v[80:81], v[84:85] op_sel_hi:[0,1]\n\t"
    "v_pk_mul_f32 v[90:91], v[82:83], v[86:87] op_sel_hi:[0,1]\n\t"
    "ds_write_addtid_b32 v98 offset:12672\n\t"
    "v_pk_mul_f32 v[92:93], v[76:77], v[84:85] op_sel_hi:[0,1]\n\t"
    "v_pk_mul_f32 v[94:95], v[78:79], v[86:87] op_sel_hi:[0,1]\n\t"
    "v_cvt_pk_bf16_f32 v96, v80, v81\n\t"
    "ds_write_addtid_b32 v99 offset:12928\n\t"
    "v_cvt_pk_bf16_f32 v97, v82, v83\n\t"
    "v_pk_fma_f32 v[80:81], v[80:81], v[84:85], v[88:89] op_sel:[1,1,0] op_sel_hi:[1,0,1] neg_lo:[0,1,0]\n\t"
    "v_pk_fma_f32 v[82:83], v[82:83], v[86:87], v[90:91] op_sel:[1,1,0] op_sel_hi:[1,0,1] neg_lo:[0,1,0]\n\t"
    "ds_write_addtid_b32 v96 offset:13200\n\t"
    "v_cvt_pk_bf16_f32 v98, v76, v77\n\t"
    "v_cvt_pk_bf16_f32 v99, v78, v79\n\t"
    "v_pk_fma_f32 v[76:77], v[76:77], v[84:85], v[92:93] op_sel:[1,1,0] op_sel_hi:[1,0,1] neg_lo:[0,1,0]\n\t"
    "ds_write_addtid_b32 v97 offset:13456\n\t"
    "v_pk_fma_f32 v[78:79], v[78:79], v[86:87], v[94:95] op_sel:[1,1,0] op_sel_hi:[1,0,1] neg_lo:[0,1,0]\n\t"
    "v_pk_mul_f32 v[88:89], v[80:81], v[84:85] op_sel_hi:[0,1]\n\t"
    "v_pk_mul_f32 v[90:91], v[82:83], v[86:87] op_sel_hi:[0,1]\n\t"
    "ds_write_addtid_b32 v98 offset:13728\n\t"
    "v_pk_mul_f32 v[92:93], v[76:77], v[84:85] op_sel_hi:[0,1]\n\t"
    "v_pk_mul_f32 v[94:95], v[78:79], v[86:87] op_sel_hi:[0,1]\n\t"
    "v_cvt_pk_bf16_f32 v96, v80, v81\n\t"
    "ds_write_addtid_b32 v99 offset:13984\n\t"
    "v_cvt_pk_bf16_f32 v97, v82, v83\n\t"
    "v_pk_fma_f32 v[80:81], v[80:81], v[84:85], v[88:89] op_sel:[1,1,0] op_sel_hi:[1,0,1] neg_lo:[0,1,0]\n\t"
    "v_pk_fma_f32 v[82:83], v[82:83], v[86:87], v[90:91] op_sel:[1,1,0] op_sel_hi:[1,0,1] neg_lo:[0,1,0]\n\t"
    "ds_write_addtid_b32 v96 offset:14256\n\t"
    "v_cvt_pk_bf16_f32 v98, v76, v77\n\t"
    "v_cvt_pk_bf16_f32 v99, v78, v79\n\t"
    "v_pk_fma_f32 v[76:77], v[76:77], v[84:85], v[92:93] op_sel:[1,1,0] op_sel_hi:[1,0,1] neg_lo:[0,1,0]\n\t"
    "ds_write_addtid_b32 v97 offset:14512\n\t"
    "v_pk_fma_f32 v[78:79], v[78:79], v[86:87], v[94:95] op_sel:[1,1,0] op_sel_hi:[1,0,1] neg_lo:[0,1,0]\n\t"
    "s_getreg_b32 s20, hwreg(HW_REG_IB_STS, 0, 4)\n\t"
    "s_cmp_eq_u32 s20, s93\n\t"
    "s_cbranch_scc0 .Lus70_%=\n\t"
    "s_swappc_b64 s[90:91], s[88:89]\n\t"
    ".Lus70_%=:\n\t"
    "v_pk_mul_f32 v[88:89], v[80:81], v[84:85] op_sel_hi:[0,1]\n\t"
    "v_pk_mul_f32 v[90:91], v[82:83], v[86:87] op_sel_hi:[0,1]\n\t"
    "ds_write_addtid_b32 v98 offset:14784\n\t"
    "v_cvt_pk_bf16_f32 v96, v80, v81\n\t"
    "ds_write_addtid_b32 v99 offset:15040\n\t"
    "v_cvt_pk_bf16_f32 v97, v82, v83\n\t"
    "v_pk_fma_f32 v[80:81], v[80:81], v[84:85], v[88:89] op_sel:[1,1,0] op_sel_hi:[1,0,1] neg_lo:[0,1,0]\n\t"
    "v_pk_fma_f32 v[82:83], v[82:83], v[86:87], v[90:91] op_sel:[1,1,0] op_sel_hi:[1,0,1] neg_lo:[0,1,0]\n\t"
    "ds_write_addtid_b32 v96 offset:15312\n\t"
    "v_cvt_pk_bf16_f32 v98, v76, v77\n\t"
    "v_cvt_pk_bf16_f32 v99, v78, v79\n\t"
    "ds_write_addtid_b32 v97 offset:15568\n\t"
    "ds_write_addtid_b32 v98 offset:15840\n\t"
    "v_cvt_pk_bf16_f32 v96, v80, v81\n\t"
    "ds_write_addtid_b32 v99 offset:16096\n\t"
    "v_cvt_pk_bf16_f32 v97, v82, v83\n\t"
    "ds_write_addtid_b32 v96 offset:16368\n\t"
    "ds_write_addtid_b32 v97 offset:16624\n\t"
    "s_getreg_b32 s20, hwreg(HW_REG_IB_STS, 0, 4)\n\t"
    "s_cmp_eq_u32 s20, s93\n\t"
    "s_cbranch_scc0 .Lus71_%=\n\t"
    "s_swappc_b64 s[90:91], s[88:89]\n\t"
    ".Lus71_%=:\n\t"
    "ds_read_b128 v[120:123], v20 offset:0\n\t"
    "v_lshlrev_b32 v104, 16, v14\n\t"
    "v_lshlrev_b32 v105, 16, v15\n\t"
    "ds_read_b128 v[124:127], v20 offset:32\n\t"
    "v_and_b32 v106, 0xffff0000, v14\n\t"
    "v_and_b32 v107, 0xffff0000, v15\n\t"
    "ds_read_b128 v[128:131], v20 offset:64\n\t"
    "v_pk_mul_f32 v[100:101], v[100:101], v[6:7] op_sel_hi:[1,0]\n\t"
    "v_pk_mul_f32 v[102:103], v[102:103], v[6:7] op_sel_hi:[1,0]\n\t"
    "ds_read_b128 v[132:135], v20 offset:96\n\t"
    "v_pk_mul_f32 v[108:109], v[104:105], v[100:101] op_sel_hi:[0,1]\n\t"
    "v_pk_mul_f32 v[110:111], v[106:107], v[102:103] op_sel_hi:[0,1]\n\t"
    "ds_read_b128 v[136:139], v20 offset:128\n\t"
    "v_pk_fma_f32 v[72:73], v[104:105], v[100:101], v[108:109] op_sel:[1,1,0] op_sel_hi:[1,0,1] neg_lo:[0,1,0] neg_hi:[0,1,1]\n\t"
    "v_pk_fma_f32 v[74:75], v[106:107], v[102:103], v[110:111] op_sel:[1,1,0] op_sel_hi:[1,0,1] neg_lo:[0,1,0] neg_hi:[0,1,1]\n\t"
    "ds_read_b128 v[140:143], v20 offset:160\n\t"
    "v_fract_f32_e64 v90, -v70\n\t"
    "v_fract_f32_e64 v91, -v71\n\t"
    "ds_read_b128 v[144:147], v20 offset:192\n\t"
    "v_exp_f32 v88, v68\n\t"
    "v_exp_f32 v89, v69\n\t"
    "ds_read_b128 v[148:151], v20 offset:224\n\t"
    "v_cos_f32 v76, v90\n\t"
    "v_sin_f32 v77, v90\n\t"
    "ds_read_b128 v[152:155], v20 offset:256\n\t"
    "v_cos_f32 v78, v91\n\t"
    "v_sin_f32 v79, v91\n\t"
    "ds_read_b128 v[156:159], v20 offset:288\n\t"
    "v_pk_mul_f32 v[76:77], v[76:77], v[88:89] op_sel_hi:[1,0]\n\t"
    "v_pk_mul_f32 v[78:79], v[78:79], v[88:89] op_sel:[0,1]\n\t"
    "ds_read_b128 v[160:163], v20 offset:320\n\t"
    "v_pk_mul_f32 v[90:91], v[70:71], s[26:27] op_sel_hi:[1,0]\n\t"
    "v_pk_mul_f32 v[88:89], v[68:69], s[26:27] op_sel_hi:[1,0]\n\t"
    "ds_read_b128 v[164:167], v20 offset:352\n\t"
    "v_fract_f32_e64 v90, -v90\n\t"
    "v_fract_f32_e64 v91, -v91\n\t"
    "ds_read_b128 v[168:171], v20 offset:384\n\t"
    "v_exp_f32 v88, v88\n\t"
    "v_exp_f32 v89, v89\n\t"
    "ds_read_b128 v[172:175], v20 offset:416\n\t"
    "v_cos_f32 v80, v90\n\t"
    "v_sin_f32 v81, v90\n\t"
    "ds_read_b128 v[176:179], v20 offset:448\n\t"
    "v_cos_f32 v82, v91\n\t"
    "v_sin_f32 v83, v91\n\t"
    "ds_read_b128 v[180:183], v20 offset:480\n\t"
    "v_pk_mul_f32 v[80:81], v[80:81], v[88:89] op_sel_hi:[1,0]\n\t"
    "v_pk_mul_f32 v[82:83], v[82:83], v[88:89] op_sel:[0,1]\n\t"
    "v_pk_mul_f32 v[88:89], v[72:73], v[76:77] op_sel_hi:[0,1]\n\t"
    "v_pk_mul_f32 v[90:91], v[74:75], v[78:79] op_sel_hi:[0,1]\n\t"
    "v_pk_mul_f32 v[92:93], v[72:73], v[80:81] op_sel_hi:[0,1]\n\t"
    "v_pk_mul_f32 v[94:95], v[74:75], v[82:83] op_sel_hi:[0,1]\n\t"
    "v_cvt_pk_bf16_f32 v96, v72, v73\n\t"
    "v_cvt_pk_bf16_f32 v97, v74, v75\n\t"
    "v_pk_fma_f32 v[112:113], v[72:73], v[76:77], v[88:89] op_sel:[1,1,0] op_sel_hi:[1,0,1] neg_lo:[0,1,0]\n\t"
    "v_pk_fma_f32 v[114:115], v[74:75], v[78:79], v[90:91] op_sel:[1,1,0] op_sel_hi:[1,0,1] neg_lo:[0,1,0]\n\t"
    "ds_write_addtid_b32 v96 offset:0\n\t"
    "v_pk_fma_f32 v[84:85], v[72:73], v[80:81], v[92:93] op_sel:[1,1,0] op_sel_hi:[1,0,1] neg_lo:[0,1,0]\n\t"
    "ds_write_addtid_b32 v97 offset:256\n\t"
    "v_pk_fma_f32 v[86:87], v[74:75], v[82:83], v[94:95] op_sel:[1,1,0] op_sel_hi:[1,0,1] neg_lo:[0,1,0]\n\t"
    "s_getreg_b32 s20, hwreg(HW_REG_IB_STS, 0, 4)\n\t"
    "s_cmp_eq_u32 s20, s93\n\t"
    "s_cbranch_scc0 .Lus72_%=\n\t"
    "s_swappc_b64 s[90:91], s[88:89]\n\t"
    ".Lus72_%=:\n\t"
    "v_pk_mul_f32 v[88:89], v[112:113], v[76:77] op_sel_hi:[0,1]\n\t"
    "v_pk_mul_f32 v[90:91], v[114:115], v[78:79] op_sel_hi:[0,1]\n\t"
    "v_pk_mul_f32 v[92:93], v[84:85], v[80:81] op_sel_hi:[0,1]\n\t"
    "v_pk_mul_f32 v[94:95], v[86:87], v[82:83] op_sel_hi:[0,1]\n\t"
    "v_cvt_pk_bf16_f32 v96, v112, v113\n\t"
    "v_cvt_pk_bf16_f32 v97, v114, v115\n\t"
    "v_pk_fma_f32 v[100:101], v[112:113], v[76:77], v[88:89] op_sel:[1,1,0] op_sel_hi:[1,0,1] neg_lo:[0,1,0]\n\t"
    "v_pk_fma_f32 v[102:103], v[114:115], v[78:79], v[90:91] op_sel:[1,1,0] op_sel_hi:[1,0,1] neg_lo:[0,1,0]\n\t"
    "ds_write_addtid_b32 v96 offset:528\n\t"
    "v_cvt_pk_bf16_f32 v98, v84, v85\n\t"
    "v_cvt_pk_bf16_f32 v99, v86, v87\n\t"
    "v_pk_fma_f32 v[84:85], v[84:85], v[80:81], v[92:93] op_sel:[1,1,0] op_sel_hi:[1,0,1] neg_lo:[0,1,0]\n\t"
    "ds_write_addtid_b32 v97 offset:784\n\t"
    "v_pk_fma_f32 v[86:87], v[86:87], v[82:83], v[94:95] op_sel:[1,1,0] op_sel_hi:[1,0,1] neg_lo:[0,1,0]\n\t"
    "v_pk_mul_f32 v[88:89], v[100:101], v[76:77] op_sel_hi:[0,1]\n\t"
    "v_pk_mul_f32 v[90:91], v[102:103], v[78:79] op_sel_hi:[0,1]\n\t"
    "ds_write_addtid_b32 v98 offset:4224\n\t"
    "v_pk_mul_f32 v[92:93], v[84:85], v[80:81] op_sel_hi:[0,1]\n\t"
    "v_pk_mul_f32 v[94:95], v[86:87], v[82:83] op_sel_hi:[0,1]\n\t"
    "v_cvt_pk_bf16_f32 v96, v100, v101\n\t"
    "ds_write_addtid_b32 v99 offset:4480\n\t"
    "v_cvt_pk_bf16_f32 v97, v102, v103\n\t"
    "v_pk_fma_f32 v[112:113], v[100:101], v[76:77], v[88:89] op_sel:[1,1,0] op_sel_hi:[1,0,1] neg_lo:[0,1,0]\n\t"
    "v_pk_fma_f32 v[114:115], v[102:103], v[78:79], v[90:91] op_sel:[1,1,0] op_sel_hi:[1,0,1] neg_lo:[0,1,0]\n\t"
    "ds_write_addtid_b32 v96 offset:1056\n\t"
    "v_cvt_pk_bf16_f32 v98, v84, v85\n\t"
    "v_cvt_pk_bf16_f32 v99, v86, v87\n\t"
    "v_pk_fma_f32 v[84:85], v[84:85], v[80:81], v[92:93] op_sel:[1,1,0] op_sel_hi:[1,0,1] neg_lo:[0,1,0]\n\t"
    "ds_write_addtid_b32 v97 offset:1312\n\t"
    "v_pk_fma_f32 v[86:87], v[86:87], v[82:83], v[94:95] op_sel:[1,1,0] op_sel_hi:[1,0,1] neg_lo:[0,1,0]\n\t"
    "s_memtime s[60:61]\n\t"
    "s_memrealtime s[62:63]\n\t"
    "s_getreg_b32 s20, hwreg(HW_REG_IB_STS, 0, 4)\n\t"
    "s_cmp_eq_u32 s20, s93\n\t"
    "s_cbranch_scc0 .Lus73_%=\n\t"
    "s_swappc_b64 s[90:91], s[88:89]\n\t"
    ".Lus73_%=:\n\t"
    "v_pk_mul_f32 v[88:89], v[112:113], v[76:77] op_sel_hi:[0,1]\n\t"
    "v_pk_mul_f32 v[90:91], v[114:115], v[78:79] op_sel_hi:[0,1]\n\t"
    "ds_write_addtid_b32 v98 offset:5280\n\t"
    "v_pk_mul_f32 v[92:93], v[84:85], v[80:81] op_sel_hi:[0,1]\n\t"
    "v_pk_mul_f32 v[94:95], v[86:87], v[82:83] op_sel_hi:[0,1]\n\t"
    "v_cvt_pk_bf16_f32 v96, v112, v113\n\t"
    "ds_write_addtid_b32 v99 offset:5536\n\t"
    "v_cvt_pk_bf16_f32 v97, v114, v115\n\t"
    "v_pk_fma_f32 v[104:105], v[112:113], v[76:77], v[88:89] op_sel:[1,1,0] op_sel_hi:[1,0,1] neg_lo:[0,1,0]\n\t"
    "v_pk_fma_f32 v[106:107], v[114:115], v[78:79], v[90:91] op_sel:[1,1,0] op_sel_hi:[1,0,1] neg_lo:[0,1,0]\n\t"
    "ds_write_addtid_b32 v96 offset:1584\n\t"
    "v_cvt_pk_bf16_f32 v98, v84, v85\n\t"
    "v_cvt_pk_bf16_f32 v99, v86, v87\n\t"
    "v_pk_fma_f32 v[84:85], v[84:85], v[80:81], v[92:93] op_sel:[1,1,0] op_sel_hi:[1,0,1] neg_lo:[0,1,0]\n\t"
    "ds_write_addtid_b32 v97 offset:1840\n\t"
    "v_pk_fma_f32 v[86:87], v[86:87], v[82:83], v[94:95] op_sel:[1,1,0] op_sel_hi:[1,0,1] neg_lo:[0,1,0]\n\t"
    "v_pk_mul_f32 v[88:89], v[104:105], v[76:77] op_sel_hi:[0,1]\n\t"
    "v_pk_mul_f32 v[90:91], v[106:107], v[78:79] op_sel_hi:[0,1]\n\t"
    "ds_write_addtid_b32 v98 offset:5808\n\t"
    "v_pk_mul_f32 v[92:93], v[84:85], v[80:81] op_sel_hi:[0,1]\n\t"
    "v_pk_mul_f32 v[94:95], v[86:87], v[82:83] op_sel_hi:[0,1]\n\t"
    "v_cvt_pk_bf16_f32 v96, v104, v105\n\t"
    "ds_write_addtid_b32 v99 offset:6064\n\t"
    "v_cvt_pk_bf16_f32 v97, v106, v107\n\t"
    "v_pk_fma_f32 v[112:113], v[104:105], v[76:77], v[88:89] op_sel:[1,1,0] op_sel_hi:[1,0,1] neg_lo:[0,1,0]\n\t"
    "v_pk_fma_f32 v[114:115], v[106:107], v[78:79], v[90:91] op_sel:[1,1,0] op_sel_hi:[1,0,1] neg_lo:[0,1,0]\n\t"
    "ds_write_addtid_b32 v96 offset:2112\n\t"
    "v_cvt_pk_bf16_f32 v98, v84, v85\n\t"
    "v_cvt_pk_bf16_f32 v99, v86, v87\n\t"
    "v_pk_fma_f32 v[84:85], v[84:85], v[80:81], v[92:93] op_sel:[1,1,0] op_sel_hi:[1,0,1] neg_lo:[0,1,0]\n\t"
    "ds_write_addtid_b32 v97 offset:2368\n\t"
    "v_pk_fma_f32 v[86:87], v[86:87], v[82:83], v[94:95] op_sel:[1,1,0] op_sel_hi:[1,0,1] neg_lo:[0,1,0]\n\t"
    "s_getreg_b32 s20, hwreg(HW_REG_IB_STS, 0, 4)\n\t"
    "s_cmp_eq_u32 s20, s93\n\t"
    "s_cbranch_scc0 .Lus74_%=\n\t"
    "s_swappc_b64 s[90:91], s[88:89]\n\t"
    ".Lus74_%=:\n\t"
    "v_pk_mul_f32 v[88:89], v[112:113], v[76:77] op_sel_hi:[0,1]\n\t"
    "v_pk_mul_f32 v[90:91], v[114:115], v[78:79] op_sel_hi:[0,1]\n\t"
    "ds_write_addtid_b32 v98 offset:8448\n\t"
    "v_pk_mul_f32 v[92:93], v[84:85], v[80:81] op_sel_hi:[0,1]\n\t"
    "v_pk_mul_f32 v[94:95], v[86:87], v[82:83] op_sel_hi:[0,1]\n\t"
    "v_cvt_pk_bf16_f32 v96, v112, v113\n\t"
    "ds_write_addtid_b32 v99 offset:8704\n\t"
    "v_cvt_pk_bf16_f32 v97, v114, v115\n\t"
    "v_pk_fma_f32 v[108:109], v[112:113], v[76:77], v[88:89] op_sel:[1,1,0] op_sel_hi:[1,0,1] neg_lo:[0,1,0]\n\t"
    "v_pk_fma_f32 v[110:111], v[114:115], v[78:79], v[90:91] op_sel:[1,1,0] op_sel_hi:[1,0,1] neg_lo:[0,1,0]\n\t"
    "ds_write_addtid_b32 v96 offset:2640\n\t"
    "v_cvt_pk_bf16_f32 v98, v84, v85\n\t"
    "v_cvt_pk_bf16_f32 v99, v86, v87\n\t"
    "v_pk_fma_f32 v[84:85], v[84:85], v[80:81], v[92:93] op_sel:[1,1,0] op_sel_hi:[1,0,1] neg_lo:[0,1,0]\n\t"
    "ds_write_addtid_b32 v97 offset:2896\n\t"
    "v_pk_fma_f32 v[86:87], v[86:87], v[82:83], v[94:95] op_sel:[1,1,0] op_sel_hi:[1,0,1] neg_lo:[0,1,0]\n\t"
    "v_pk_mul_f32 v[88:89], v[108:109], v[76:77] op_sel_hi:[0,1]\n\t"
    "v_pk_mul_f32 v[90:91], v[110:111], v[78:79] op_sel_hi:[0,1]\n\t"
    "ds_write_addtid_b32 v98 offset:8976\n\t"
    "v_pk_mul_f32 v[92:93], v[84:85], v[80:81] op_sel_hi:[0,1]\n\t"
    "v_pk_mul_f32 v[94:95], v[86:87], v[82:83] op_sel_hi:[0,1]\n\t"
    "v_cvt_pk_bf16_f32 v96, v108, v109\n\t"
    "ds_write_addtid_b32 v99 offset:9232\n\t"
    "v_cvt_pk_bf16_f32 v97, v110, v111\n\t"
    "v_pk_fma_f32 v[112:113], v[108:109], v[76:77], v[88:89] op_sel:[1,1,0] op_sel_hi:[1,0,1] neg_lo:[0,1,0]\n\t"
    "v_pk_fma_f32 v[114:115], v[110:111], v[78:79], v[90:91] op_sel:[1,1,0] op_sel_hi:[1,0,1] neg_lo:[0,1,0]\n\t"
    "ds_write_addtid_b32 v96 offset:3168\n\t"
    "v_cvt_pk_bf16_f32 v98, v84, v85\n\t"
    "v_cvt_pk_bf16_f32 v99, v86, v87\n\t"
    "v_pk_fma_f32 v[84:85], v[84:85], v[80:81], v[92:93] op_sel:[1,1,0] op_sel_hi:[1,0,1] neg_lo:[0,1,0]\n\t"
    "ds_write_addtid_b32 v97 offset:3424\n\t"
    "v_pk_fma_f32 v[86:87], v[86:87], v[82:83], v[94:95] op_sel:[1,1,0] op_sel_hi:[1,0,1] neg_lo:[0,1,0]\n\t"
    "s_mul_i32 s21, s15, 99\n\t"
    "s_min_u32 s93, s93, s21\n\t"
    "s_getreg_b32 s20, hwreg(HW_REG_IB_STS, 0, 4)\n\t"
    "s_cmp_eq_u32 s20, s93\n\t"
    "s_cbranch_scc0 .Lus75_%=\n\t"
    "s_swappc_b64 s[90:91], s[88:89]\n\t"
    ".Lus75_%=:\n\t"
    "v_pk_mul_f32 v[88:89], v[104:105], v[80:81] op_sel_hi:[0,1]\n\t"
    "v_pk_mul_f32 v[90:91], v[106:107], v[82:83] op_sel_hi:[0,1]\n\t"
    "ds_write_addtid_b32 v98 offset:10560\n\t"
    "v_pk_mul_f32 v[92:93], v[84:85], v[80:81] op_sel_hi:[0,1]\n\t"
    "v_pk_mul_f32 v[94:95], v[86:87], v[82:83] op_sel_hi:[0,1]\n\t"
    "v_cvt_pk_bf16_f32 v96, v112, v113\n\t"
    "ds_write_addtid_b32 v99 offset:10816\n\t"
    "v_cvt_pk_bf16_f32 v97, v114, v115\n\t"
    "v_pk_fma_f32 v[104:105], v[104:105], v[80:81], v[88:89] op_sel:[1,1,0] op_sel_hi:[1,0,1] neg_lo:[0,1,0]\n\t"
    "v_pk_fma_f32 v[106:107], v[106:107], v[82:83], v[90:91] op_sel:[1,1,0] op_sel_hi:[1,0,1] neg_lo:[0,1,0]\n\t"
    "ds_write_addtid_b32 v96 offset:3696\n\t"
    "v_cvt_pk_bf16_f32 v98, v84, v85\n\t"
    "v_cvt_pk_bf16_f32 v99, v86, v87\n\t"
    "v_pk_fma_f32 v[84:85], v[84:85], v[80:81], v[92:93] op_sel:[1,1,0] op_sel_hi:[1,0,1] neg_lo:[0,1,0]\n\t"
    "ds_write_addtid_b32 v97 offset:3952\n\t"
    "v_pk_fma_f32 v[86:87], v[86:87], v[82:83], v[94:95] op_sel:[1,1,0] op_sel_hi:[1,0,1] neg_lo:[0,1,0]\n\t"
    "v_pk_mul_f32 v[88:89], v[104:105], v[80:81] op_sel_hi:[0,1]\n\t"
    "v_pk_mul_f32 v[90:91], v[106:107], v[82:83] op_sel_hi:[0,1]\n\t"
    "ds_write_addtid_b32 v98 offset:11088\n\t"
    "v_pk_mul_f32 v[92:93], v[100:101], v[80:81] op_sel_hi:[0,1]\n\t"
    "v_pk_mul_f32 v[94:95], v[102:103], v[82:83] op_sel_hi:[0,1]\n\t"
    "v_cvt_pk_bf16_f32 v96, v104, v105\n\t"
    "ds_write_addtid_b32 v99 offset:11344\n\t"
    "v_cvt_pk_bf16_f32 v97, v106, v107\n\t"
    "v_pk_fma_f32 v[104:105], v[104:105], v[80:81], v[88:89] op_sel:[1,1,0] op_sel_hi:[1,0,1] neg_lo:[0,1,0]\n\t"
    "v_pk_fma_f32 v[106:107], v[106:107], v[82:83], v[90:91] op_sel:[1,1,0] op_sel_hi:[1,0,1] neg_lo:[0,1,0]\n\t"
    "ds_write_addtid_b32 v96 offset:6336\n\t"
    "v_cvt_pk_bf16_f32 v98, v84, v85\n\t"
    "v_cvt_pk_bf16_f32 v99, v86, v87\n\t"
    "v_pk_fma_f32 v[100:101], v[100:101], v[80:81], v[92:93] op_sel:[1,1,0] op_sel_hi:[1,0,1] neg_lo:[0,1,0]\n\t"
    "ds_write_addtid_b32 v97 offset:6592\n\t"
    "v_pk_fma_f32 v[102:103], v[102:103], v[82:83], v[94:95] op_sel:[1,1,0] op_sel_hi:[1,0,1] neg_lo:[0,1,0]\n\t"
    "s_getreg_b32 s20, hwreg(HW_REG_IB_STS, 0, 4)\n\t"
    "s_cmp_eq_u32 s20, s93\n\t"
    "s_cbranch_scc0 .Lus76_%=\n\t"
    "s_swappc_b64 s[90:91], s[88:89]\n\t"
    ".Lus76_%=:\n\t"
    "v_pk_mul_f32 v[88:89], v[104:105], v[80:81] op_sel_hi:[0,1]\n\t"
    "v_pk_mul_f32 v[90:91], v[106:107], v[82:83] op_sel_hi:[0,1]\n\t"
    "ds_write_addtid_b32 v98 offset:9504\n\t"
    "v_pk_mul_f32 v[92:93], v[108:109], v[80:81] op_sel_hi:[0,1]\n\t"
    "v_pk_mul_f32 v[94:95], v[110:111], v[82:83] op_sel_hi:[0,1]\n\t"
    "v_cvt_pk_bf16_f32 v96, v104, v105\n\t"
    "ds_write_addtid_b32 v99 offset:9760\n\t"
    "v_cvt_pk_bf16_f32 v97, v106, v107\n\t"
    "v_pk_fma_f32 v[104:105], v[104:105], v[80:81], v[88:89] op_sel:[1,1,0] op_sel_hi:[1,0,1] neg_lo:[0,1,0]\n\t"
    "v_pk_fma_f32 v[106:107], v[106:107], v[82:83], v[90:91] op_sel:[1,1,0] op_sel_hi:[1,0,1] neg_lo:[0,1,0]\n\t"
    "ds_write_addtid_b32 v96 offset:7392\n\t"
    "v_cvt_pk_bf16_f32 v98, v100, v101\n\t"
    "v_cvt_pk_bf16_f32 v99, v102, v103\n\t"
    "v_pk_fma_f32 v[108:109], v[108:109], v[80:81], v[92:93] op_sel:[1,1,0] op_sel_hi:[1,0,1] neg_lo:[0,1,0]\n\t"
    "ds_write_addtid_b32 v97 offset:7648\n\t"
    "v_pk_fma_f32 v[110:111], v[110:111], v[82:83], v[94:95] op_sel:[1,1,0] op_sel_hi:[1,0,1] neg_lo:[0,1,0]\n\t"
    "ds_write_addtid_b32 v98 offset:4752\n\t"
    "v_cvt_pk_bf16_f32 v96, v104, v105\n\t"
    "ds_write_addtid_b32 v99 offset:5008\n\t"
    "v_cvt_pk_bf16_f32 v97, v106, v107\n\t"
    "ds_write_addtid_b32 v96 offset:7920\n\t"
    "v_cvt_pk_bf16_f32 v98, v108, v109\n\t"
    "v_cvt_pk_bf16_f32 v99, v110, v111\n\t"
    "ds_write_addtid_b32 v97 offset:8176\n\t"
    "ds_write_addtid_b32 v98 offset:6864\n\t"
    "ds_write_addtid_b32 v99 offset:7120\n\t"
    "ds_read_b128 v[184:187], v20 offset:0\n\t"
    "ds_read_b128 v[188:191], v20 offset:32\n\t"
    "ds_read_b128 v[192:195], v20 offset:64\n\t"
    "ds_read_b128 v[196:199], v20 offset:96\n\t"
    "ds_read_b128 v[200:203], v20 offset:128\n\t"
    "ds_read_b128 v[204:207], v20 offset:160\n\t"
    "ds_read_b128 v[208:211], v20 offset:192\n\t"
    "ds_read_b128 v[212:215], v20 offset:224\n\t"
    "ds_read_b128 v[216:219], v20 offset:256\n\t"
    "ds_read_b128 v[220:223], v20 offset:288\n\t"
    "ds_read_b128 v[224:227], v20 offset:320\n\t"
    "ds_read_b128 v[228:231], v20 offset:352\n\t"
    "ds_read_b128 v[232:235], v20 offset:384\n\t"
    "ds_read_b128 v[236:239], v20 offset:416\n\t"
    "ds_read_b128 v[240:243], v20 offset:448\n\t"
    "ds_read_b128 v[244:247], v20 offset:480\n\t"
    "s_cmp_eq_u32 s8, 7\n\t"
    "s_cbranch_scc0 .Lsk77_%=\n\t"
    "s_memrealtime s[80:81]\n\t"
    "s_waitcnt lgkmcnt(0)\n\t"
    "s_bfe_u32 s83, s80, 196613\n\t"
    "s_lshl_b32 s83, s83, 7\n\t"
    "buffer_load_dwordx3 v[32:34], v4, s[48:51], s83 offen offset:128 sc1\n\t"
    ".Lsk77_%=:\n\t"
    "s_getreg_b32 s20, hwreg(HW_REG_IB_STS, 0, 4)\n\t"
    "s_cmp_eq_u32 s20, s93\n\t"
    "s_cbranch_scc0 .Lus78_%=\n\t"
    "s_swappc_b64 s[90:91], s[88:89]\n\t"
    ".Lus78_%=:\n\t"
    "s_cmp_lg_u32 s15, 0\n\t"
    "s_cbranch_scc0 .Lp379_%=x\n\t"
    "s_setprio 2\n\t"
    ".Lp379_%=x:\n\t"
    "s_cmp_eq_u32 s8, 7\n\t"
    "s_cbranch_scc1 .Lrd81_%=\n\t"
    "s_waitcnt lgkmcnt(15)\n\t"
    "v_mfma_f32_32x32x16_bf16 v[52:67], v[184:187], v[120:123], v[52:67]\n\t"
    "s_waitcnt lgkmcnt(14)\n\t"
    "v_mfma_f32_32x32x16_bf16 v[52:67], v[188:191], v[124:127], v[52:67]\n\t"
    "s_waitcnt lgkmcnt(13)\n\t"
    "v_mfma_f32_32x32x16_bf16 v[52:67], v[192:195], v[128:131], v[52:67]\n\t"
    "s_waitcnt lgkmcnt(12)\n\t"
    "v_mfma_f32_32x32x16_bf16 v[52:67], v[196:199], v[132:135], v[52:67]\n\t"
    "s_waitcnt lgkmcnt(11)\n\t"
    "v_mfma_f32_32x32x16_bf16 v[52:67], v[200:203], v[136:139], v[52:67]\n\t"
    "s_waitcnt lgkmcnt(10)\n\t"
    "v_mfma_f32_32x32x16_bf16 v[52:67], v[204:207], v[140:143], v[52:67]\n\t"
    "s_waitcnt lgkmcnt(9)\n\t"
    "v_mfma_f32_32x32x16_bf16 v[52:67], v[208:211], v[144:147], v[52:67]\n\t"
    "s_waitcnt lgkmcnt(8)\n\t"
    "v_mfma_f32_32x32x16_bf16 v[52:67], v[212:215], v[148:151], v[52:67]\n\t"
    "s_waitcnt lgkmcnt(7)\n\t"
    "v_mfma_f32_32x32x16_bf16 v[52:67], v[216:219], v[152:155], v[52:67]\n\t"
    "s_waitcnt lgkmcnt(6)\n\t"
    "v_mfma_f32_32x32x16_bf16 v[52:67], v[220:223], v[156:159], v[52:67]\n\t"
    "s_waitcnt lgkmcnt(5)\n\t"
    "v_mfma_f32_32x32x16_bf16 v[52:67], v[224:227], v[160:163], v[52:67]\n\t"
    "s_waitcnt lgkmcnt(4)\n\t"
    "v_mfma_f32_32x32x16_bf16 v[52:67], v[228:231], v[164:167], v[52:67]\n\t"
    "s_waitcnt lgkmcnt(3)\n\t"
    "v_mfma_f32_32x32x16_bf16 v[52:67], v[232:235], v[168:171], v[52:67]\n\t"
    "s_waitcnt lgkmcnt(2)\n\t"
    "v_mfma_f32_32x32x16_bf16 v[52:67], v[236:239], v[172:175], v[52:67]\n\t"
    "s_waitcnt lgkmcnt(1)\n\t"
    "v_mfma_f32_32x32x16_bf16 v[52:67], v[240:243], v[176:179], v[52:67]\n\t"
    "s_waitcnt lgkmcnt(0)\n\t"
    "v_mfma_f32_32x32x16_bf16 v[52:67], v[244:247], v[180:183], v[52:67]\n\t"
    "s_lshl2_add_u32 s24, s12, 153856\n\t"
    "v_mov_b32 v10, s24\n\t"
    "s_cmp_lg_u32 s15, 0\n\t"
    "s_cbranch_scc0 .Lp379_%=\n\t"
    "s_setprio 0\n\t"
    "v_lshl_add_u32 v22, v1, 4, s18\n\t"
    "v_mov_b32 v11, 1\n\t"
    "s_nop 4\n\t"
    "ds_write_b128 v22, v[52:55] offset:0\n\t"
    "ds_write_b128 v22, v[56:59] offset:1024\n\t"
    "ds_write_b128 v22, v[60:63] offset:2048\n\t"
    "ds_write_b32 v10, v11\n\t"
    "s_branch .Lem80_%=\n\t"
    ".Lrd81_%=:\n\t"
    "s_waitcnt lgkmcnt(15)\n\t"
    "v_mfma_f32_32x32x16_bf16 v[52:67], v[184:187], v[120:123], v[52:67]\n\t"
    "v_lshlrev_b32 v12, 4, v1\n\t"
    "s_and_b32 s22, s9, 1\n\t"
    "v_lshrrev_b32 v15, 4, v1\n\t"
    "s_waitcnt lgkmcnt(14)\n\t"
    "v_mfma_f32_32x32x16_bf16 v[52:67], v[188:191], v[124:127], v[52:67]\n\t"
    "s_xor_b32 s22, s22, 1\n\t"
    "v_and_b32 v14, 0xf0, v12\n\t"
    "s_lshr_b32 s23, s9, 1\n\t"
    "s_waitcnt lgkmcnt(13)\n\t"
    "v_mfma_f32_32x32x16_bf16 v[52:67], v[192:195], v[128:131], v[52:67]\n\t"
    "v_lshl_add_u32 v14, v15, 15, v14\n\t"
    "s_lshl2_add_u32 s22, s22, s23\n\t"
    "s_lshl4_add_u32 s22, s22, s11\n\t"
    "s_waitcnt lgkmcnt(12)\n\t"
    "v_mfma_f32_32x32x16_bf16 v[52:67], v[196:199], v[132:135], v[52:67]\n\t"
    "s_lshl_b32 s30, s22, 8\n\t"
    "s_add_u32 s30, s30, 53248\n\t"
    "v_and_b32 v5, 1, v1\n\t"
    "s_waitcnt lgkmcnt(11)\n\t"
    "v_mfma_f32_32x32x16_bf16 v[52:67], v[200:203], v[136:139], v[52:67]\n\t"
    "v_bfe_u32 v13, v1, 1, 2\n\t"
    "v_lshlrev_b32 v5, 4, v5\n\t"
    "s_movk_i32 s20, 0x3f80\n\t"
    "s_waitcnt lgkmcnt(10)\n\t"
    "v_mfma_f32_32x32x16_bf16 v[52:67], v[204:207], v[140:143], v[52:67]\n\t"
    "v_lshlrev_b32_e64 v5, v5, s20\n\t"
    "v_cmp_eq_u32 vcc, 0, v13\n\t"
    "s_nop 1\n\t"
    "v_cndmask_b32 v24, 0, v5, vcc\n\t"
    "s_waitcnt lgkmcnt(9)\n\t"
    "v_mfma_f32_32x32x16_bf16 v[52:67], v[208:211], v[144:147], v[52:67]\n\t"
    "v_cmp_eq_u32 vcc, 1, v13\n\t"
    "s_nop 1\n\t"
    "v_cndmask_b32 v25, 0, v5, vcc\n\t"
    "v_cmp_eq_u32 vcc, 2, v13\n\t"
    "s_waitcnt lgkmcnt(8)\n\t"
    "v_mfma_f32_32x32x16_bf16 v[52:67], v[212:215], v[148:151], v[52:67]\n\t"
    "v_cndmask_b32 v26, 0, v5, vcc\n\t"
    "v_cmp_eq_u32 vcc, 3, v13\n\t"
    "s_nop 1\n\t"
    "v_cndmask_b32 v27, 0, v5, vcc\n\t"
    "s_waitcnt lgkmcnt(7)\n\t"
    "v_mfma_f32_32x32x16_bf16 v[52:67], v[216:219], v[152:155], v[52:67]\n\t"
    "v_lshlrev_b32 v16, 4, v1\n\t"
    "v_lshrrev_b64 v[16:17], v16, s[78:79]\n\t"
    "v_lshlrev_b32 v16, 16, v16\n\t"
    "s_waitcnt lgkmcnt(6)\n\t"
    "v_mfma_f32_32x32x16_bf16 v[52:67], v[220:223], v[156:159], v[52:67]\n\t"
    "s_mov_b32 s27, 0xffff\n\t"
    "s_lshl_b32 s22, s22, 13\n\t"
    "s_add_u32 s22, s22, 184320\n\t"
    "s_waitcnt lgkmcnt(5)\n\t"
    "v_mfma_f32_32x32x16_bf16 v[52:67], v[224:227], v[160:163], v[52:67]\n\t"
    "s_add_u32 s23, s22, 4096\n\t"
    "s_movk_i32 s20, 2950\n\t"
    "s_cmp_eq_u32 s9, 7\n\t"
    "s_waitcnt lgkmcnt(4)\n\t"
    "v_mfma_f32_32x32x16_bf16 v[52:67], v[228:231], v[164:167], v[52:67]\n\t"
    "s_cselect_b32 s20, 3245, s20\n\t"
    "s_lshl2_add_u32 s24, s12, 153856\n\t"
    "v_mov_b32 v10, s24\n\t"
    "s_waitcnt lgkmcnt(3)\n\t"
    "v_mfma_f32_32x32x16_bf16 v[52:67], v[232:235], v[168:171], v[52:67]\n\t"
    "v_lshl_add_u32 v22, v1, 4, s18\n\t"
    "v_mov_b32 v11, 1\n\t"
    "s_waitcnt lgkmcnt(2)\n\t"
    "v_mfma_f32_32x32x16_bf16 v[52:67], v[236:239], v[172:175], v[52:67]\n\t"
    "s_waitcnt lgkmcnt(1)\n\t"
    "v_mfma_f32_32x32x16_bf16 v[52:67], v[240:243], v[176:179], v[52:67]\n\t"
    "s_cmp_ge_u32 s9, 7\n\t"
    "s_cbranch_scc1 .Lrd81_%=g\n\t"
    "s_waitcnt lgkmcnt(0)\n\t"
    "v_mfma_f32_32x32x16_bf16 v[52:67], v[244:247], v[180:183], v[52:67]\n\t"
    "s_memtime s[84:85]\n\t"
    "s_memrealtime s[86:87]\n\t"
    "s_waitcnt vmcnt(0)\n\t"
    "v_readfirstlane_b32 s80, v32\n\t"
    "v_readfirstlane_b32 s81, v33\n\t"
    "v_readfirstlane_b32 s82, v34\n\t"
    "s_cmp_eq_u64 s[80:81], s[78:79]\n\t"
    "s_cselect_b32 s31, 1, 0\n\t"
    "s_sub_u32 s80, s82, s62\n\t"
    "s_mul_i32 s80, s80, 18\n\t"
    "s_add_u32 s80, s80, s60\n\t"
    "s_waitcnt lgkmcnt(0)\n\t"
    "s_sub_u32 s84, s84, s80\n\t"
    "s_sub_u32 s86, s86, s82\n\t"
    "s_cmp_ge_u32 s84, s20\n\t"
    "s_cselect_b32 s86, s86, 0\n\t"
    "s_cmp_ge_u32 s86, 187\n\t"
    "s_cselect_b32 s86, s31, 0\n\t"
    "s_cmp_eq_u32 s86, 0\n\t"
    "s_cbranch_scc1 .Lrd81_%=i\n\t"
    "buffer_load_dwordx4 v[212:215], v12, s[48:51], s22 offen offset:0 sc1\n\t"
    "buffer_load_dwordx4 v[216:219], v12, s[48:51], s22 offen offset:1024 sc1\n\t"
    "buffer_load_dwordx4 v[220:223], v12, s[48:51], s22 offen offset:2048 sc1\n\t"
    "buffer_load_dwordx4 v[224:227], v12, s[48:51], s22 offen offset:3072 sc1\n\t"
    "buffer_load_dwordx4 v[228:231], v12, s[48:51], s23 offen offset:0 sc1\n\t"
    "buffer_load_dwordx4 v[232:235], v12, s[48:51], s23 offen offset:1024 sc1\n\t"
    "buffer_load_dwordx4 v[236:239], v12, s[48:51], s23 offen offset:2048 sc1\n\t"
    "buffer_load_dwordx4 v[240:243], v12, s[48:51], s23 offen offset:3072 sc1\n\t"
    ".Lrd81_%=i:\n\t"
    "ds_write_b128 v22, v[52:55] offset:0\n\t"
    "ds_write_b128 v22, v[56:59] offset:1024\n\t"
    "ds_write_b128 v22, v[60:63] offset:2048\n\t"
    "ds_write_b32 v10, v11\n\t"
    "s_cmp_lg_u32 s86, 0\n\t"
    "s_cbranch_scc1 .Lrd81_%=c\n\t"
    "s_cmp_lg_u32 s31, 0\n\t"
    "s_cbranch_scc1 .Lrd81_%=h\n\t"
    ".Lrd81_%=g:\n\t"
    "s_setprio 0\n\t"
    "s_mov_b32 s34, 8\n\t"
    ".Lgl82_%=:\n\t"
    "s_memrealtime s[80:81]\n\t"
    "s_waitcnt lgkmcnt(0)\n\t"
    "s_bfe_u32 s83, s80, 196613\n\t"
    "s_lshl_b32 s83, s83, 7\n\t"
    "s_sub_u32 s34, s34, 1\n\t"
    "s_cbranch_scc1 .Lgl82_%=v\n\t"
    "s_buffer_load_dwordx4 s[80:83], s[48:51], s83 offset:128\n\t"
    "s_waitcnt lgkmcnt(0)\n\t"
    "s_branch .Lgs84_%=\n\t"
    ".Lgl82_%=v:\n\t"
    "buffer_load_dwordx3 v[20:22], v4, s[48:51], s83 offen offset:128 sc1\n\t"
    "s_mov_b32 s34, 0\n\t"
    "s_waitcnt vmcnt(0)\n\t"
    "v_readfirstlane_b32 s80, v20\n\t"
    "v_readfirstlane_b32 s81, v21\n\t"
    "v_readfirstlane_b32 s82, v22\n\t"
    ".Lgs84_%=:\n\t"
    "s_cmp_eq_u64 s[80:81], s[78:79]\n\t"
    "s_cbranch_scc1 .Lgo83_%=\n\t"
    "s_sleep 2\n\t"
    "s_add_u32 s35, s35, 16\n\t"
    "s_cmp_lt_u32 s35, 0x400000\n\t"
    "s_cbranch_scc1 .Lgl82_%=\n\t"
    ".Lgo83_%=:\n\t"
    ".Lrd81_%=h:\n\t"
    "s_setprio 3\n\t"
    ".Lu85_%=:\n\t"
    "s_memrealtime s[80:81]\n\t"
    "s_waitcnt lgkmcnt(0)\n\t"
    "s_sub_u32 s80, s80, s82\n\t"
    "s_cmp_lt_u32 s80, 187\n\t"
    "s_cbranch_scc1 .Lu85_%=\n\t"
    "s_sub_u32 s82, s82, s62\n\t"
    "s_mul_i32 s82, s82, 18\n\t"
    "s_add_u32 s82, s82, s60\n\t"
    "s_memtime s[80:81]\n\t"
    "s_waitcnt lgkmcnt(0)\n\t"
    "s_sub_u32 s80, s80, s82\n\t"
    "s_sub_u32 s80, s20, s80\n\t"
    "s_cbranch_scc1 .Lrd81_%=e\n\t"
    "s_mul_i32 s80, s80, 1\n\t"
    "s_min_u32 s80, s80, 0\n\t"
    "s_add_u32 s20, s20, s80\n\t"
    ".Lu86_%=:\n\t"
    "s_memtime s[80:81]\n\t"
    "s_waitcnt lgkmcnt(0)\n\t"
    "s_sub_u32 s80, s80, s82\n\t"
    "s_cmp_lt_u32 s80, s20\n\t"
    "s_cbranch_scc1 .Lu86_%=\n\t"
    ".Lrd81_%=e:\n\t"
    ".Lrd81_%=a:\n\t"
    "buffer_load_dwordx4 v[212:215], v12, s[48:51], s22 offen offset:0 sc1\n\t"
    "buffer_load_dwordx4 v[216:219], v12, s[48:51], s22 offen offset:1024 sc1\n\t"
    "buffer_load_dwordx4 v[220:223], v12, s[48:51], s22 offen offset:2048 sc1\n\t"
    "buffer_load_dwordx4 v[224:227], v12, s[48:51], s22 offen offset:3072 sc1\n\t"
    "buffer_load_dwordx4 v[228:231], v12, s[48:51], s23 offen offset:0 sc1\n\t"
    "buffer_load_dwordx4 v[232:235], v12, s[48:51], s23 offen offset:1024 sc1\n\t"
    "buffer_load_dwordx4 v[236:239], v12, s[48:51], s23 offen offset:2048 sc1\n\t"
    "buffer_load_dwordx4 v[240:243], v12, s[48:51], s23 offen offset:3072 sc1\n\t"
    ".Lrd81_%=c:\n\t"
    "s_add_u32 s35, s35, 16\n\t"
    "s_cmp_lt_u32 s35, 0x400000\n\t"
    "s_waitcnt vmcnt(7)\n\t"
    "v_xor_b32 v215, v215, v16\n\t"
    "s_waitcnt vmcnt(6)\n\t"
    "v_xor_b32 v219, v219, v16\n\t"
    "v_or_b32 v32, v215, v219\n\t"
    "v_mfma_f32_16x16x32_bf16 v[28:31], v[24:27], v[212:215], 0\n\t"
    "s_waitcnt vmcnt(5)\n\t"
    "v_xor_b32 v223, v223, v16\n\t"
    "v_mfma_f32_16x16x32_bf16 v[28:31], v[24:27], v[216:219], v[28:31]\n\t"
    "s_waitcnt vmcnt(4)\n\t"
    "v_xor_b32 v227, v227, v16\n\t"
    "v_or3_b32 v32, v32, v223, v227\n\t"
    "v_mfma_f32_16x16x32_bf16 v[28:31], v[24:27], v[220:223], v[28:31]\n\t"
    "s_waitcnt vmcnt(3)\n\t"
    "v_xor_b32 v231, v231, v16\n\t"
    "v_mfma_f32_16x16x32_bf16 v[28:31], v[24:27], v[224:227], v[28:31]\n\t"
    "s_waitcnt vmcnt(2)\n\t"
    "v_xor_b32 v235, v235, v16\n\t"
    "v_or3_b32 v32, v32, v231, v235\n\t"
    "v_mfma_f32_16x16x32_bf16 v[28:31], v[24:27], v[228:231], v[28:31]\n\t"
    "s_waitcnt vmcnt(1)\n\t"
    "v_xor_b32 v239, v239, v16\n\t"
    "v_mfma_f32_16x16x32_bf16 v[28:31], v[24:27], v[232:235], v[28:31]\n\t"
    "s_waitcnt vmcnt(0)\n\t"
    "v_xor_b32 v243, v243, v16\n\t"
    "v_or3_b32 v32, v32, v239, v243\n\t"
    "v_mfma_f32_16x16x32_bf16 v[28:31], v[24:27], v[236:239], v[28:31]\n\t"
    "v_cmp_lt_u32 vcc, s27, v32\n\t"
    "v_mfma_f32_16x16x32_bf16 v[28:31], v[24:27], v[240:243], v[28:31]\n\t"
    "s_cselect_b64 vcc, vcc, 0\n\t"
    "s_cbranch_vccz .Lrd81_%=b\n\t"
    "s_sleep 2\n\t"
    "s_branch .Lrd81_%=a\n\t"
    ".Lrd81_%=b:\n\t"
    "s_nop 3\n\t"
    "v_cvt_pk_bf16_f32 v32, v28, v29\n\t"
    "v_cvt_pk_bf16_f32 v33, v30, v31\n\t"
    "v_cvt_pk_bf16_f32 v34, v28, v29\n\t"
    "v_cvt_pk_bf16_f32 v35, v30, v31\n\t"
    "s_nop 0\n\t"
    "v_permlane16_swap_b32 v32, v34\n\t"
    "v_permlane16_swap_b32 v33, v35\n\t"
    "v_and_or_b32 v35, v35, s27, v16\n\t"
    "s_mov_b32 exec_lo, 0xffff0000\n\t"
    "buffer_store_dwordx4 v[32:35], v14, s[48:51], s30 offen sc1\n\t"
    "s_mov_b64 exec, 0xffff\n\t"
    "buffer_store_dwordx4 v[32:35], v14, s[48:51], s30 offen sc1\n\t"
    "s_mov_b64 exec, -1\n\t"
    "s_setprio 0\n\t"
    "s_cmp_ge_u32 s9, 7\n\t"
    "s_cbranch_scc1 .Lq259_%=\n\t"
    "v_mov_b32 v11, 8\n\t"
    "s_waitcnt vmcnt(0)\n\t"
    "s_sleep 4\n\t"
    "ds_write_b32 v10, v11 offset:20\n\t"
    ".Lem80_%=:\n\t"
    "v_min_u32 v15, 4, v1\n\t"
    "s_lshl_b32 s21, s12, 9\n\t"
    "v_lshlrev_b32 v15, 2, v15\n\t"
    "v_lshl_add_u32 v16, v1, 3, s21\n\t"
    "v_add_u32 v15, 153872, v15\n\t"
    "v_mul_u32_u24 v17, 0x1c00, v1\n\t"
    "s_mul_i32 s20, s12, 458752\n\t"
    "s_lshl3_add_u32 s20, s13, s20\n\t"
    "s_mov_b32 s24, 0x05040100\n\t"
    "s_mov_b32 s25, 0x07060302\n\t"
    "s_mov_b32 s22, s12\n\t"
    "v_bfe_u32 v12, v1, 2, 2\n\t"
    "v_lshrrev_b32 v13, 4, v1\n\t"
    "v_mul_u32_u24 v12, 16896, v12\n\t"
    "v_and_b32 v14, 3, v1\n\t"
    "v_lshl_add_u32 v13, v13, 2, v14\n\t"
    "v_mul_u32_u24 v17, 0x700, v1\n\t"
    "v_lshl_add_u32 v16, v13, 3, v12\n\t"
    "v_add_u32 v16, s21, v16\n\t"
    ".Lem87_%=:\n\t"
    ".Lp88_%=:\n\t"
    "ds_read_b32 v11, v15\n\t"
    "s_add_u32 s35, s35, 1\n\t"
    "s_cmp_lt_u32 s35, 0x400000\n\t"
    "s_waitcnt lgkmcnt(0)\n\t"
    "v_cmp_ge_u32 vcc, s22, v11\n\t"
    "s_cbranch_vccz .Lo89_%=\n\t"
    "s_sleep 1\n\t"
    "s_cbranch_scc1 .Lp88_%=\n\t"
    ".Lo89_%=:\n\t"
    "ds_read_b64_tr_b16 v[20:21], v16 offset:0\n\t"
    "ds_read_b64_tr_b16 v[22:23], v16 offset:128\n\t"
    "ds_read_b64_tr_b16 v[24:25], v16 offset:256\n\t"
    "ds_read_b64_tr_b16 v[26:27], v16 offset:384\n\t"
    "s_add_u32 s21, s20, 0\n\t"
    "s_waitcnt lgkmcnt(3)\n\t"
    "buffer_store_dwordx2 v[20:21], v17, s[52:55], s21 offen\n\t"
    "s_add_u32 s21, s20, 114688\n\t"
    "s_waitcnt lgkmcnt(2)\n\t"
    "buffer_store_dwordx2 v[22:23], v17, s[52:55], s21 offen\n\t"
    "s_add_u32 s21, s20, 229376\n\t"
    "s_waitcnt lgkmcnt(1)\n\t"
    "buffer_store_dwordx2 v[24:25], v17, s[52:55], s21 offen\n\t"
    "s_add_u32 s21, s20, 344064\n\t"
    "s_waitcnt lgkmcnt(0)\n\t"
    "buffer_store_dwordx2 v[26:27], v17, s[52:55], s21 offen\n\t"
    "v_add_u32 v16, 0x800, v16\n\t"
    "s_add_u32 s20, s20, 1835008\n\t"
    "s_add_u32 s22, s22, 4\n\t"
    "s_cmp_lt_u32 s22, 8\n\t"
    "s_cbranch_scc1 .Lem87_%=\n\t"
    ".Lq259_%=:\n\t"
    "s_endpgm\n\t"
    ".Lp379_%=:\n\t"
    "s_add_u32 s24, s18, 67584\n\t"
    "v_lshl_add_u32 v11, v1, 4, s24\n\t"
    ".Lp379_%=z:\n\t"
    "s_cmp_eq_u32 s22, 0\n\t"
    "s_cbranch_scc1 .Lp379_%=y\n\t"
    "s_swappc_b64 s[90:91], s[88:89]\n\t"
    "s_add_u32 s35, s35, 16\n\t"
    "s_cmp_lt_u32 s35, 0x400000\n\t"
    "s_cbranch_scc1 .Lp379_%=z\n\t"
    ".Lp379_%=y:\n\t"
    ".Lp90_%=:\n\t"
    "ds_read_b32 v12, v10\n\t"
    "s_add_u32 s35, s35, 1\n\t"
    "s_cmp_lt_u32 s35, 0x400000\n\t"
    "s_waitcnt lgkmcnt(0)\n\t"
    "v_cmp_gt_u32 vcc, 1, v12\n\t"
    "s_cbranch_vccz .Lo91_%=\n\t"
    "s_cbranch_scc1 .Lp90_%=\n\t"
    ".Lo91_%=:\n\t"
    "ds_read_b128 v[120:123], v11 offset:0\n\t"
    "ds_read_b128 v[124:127], v11 offset:1024\n\t"
    "ds_read_b128 v[128:131], v11 offset:2048\n\t"
    "s_waitcnt lgkmcnt(1)\n\t"
    "v_pk_add_f32 v[56:57], v[56:57], v[124:125]\n\t"
    "v_pk_add_f32 v[52:53], v[52:53], v[120:121]\n\t"
    "v_pk_add_f32 v[54:55], v[54:55], v[122:123]\n\t"
    "v_pk_add_f32 v[58:59], v[58:59], v[126:127]\n\t"
    "v_cvt_pk_bf16_f32 v4, v57, v56\n\t"
    "v_cvt_pk_bf16_f32 v5, v56, v56\n\t"
    "v_cvt_pk_bf16_f32 v6, v55, v54\n\t"
    "ds_write_b64 v26, v[4:5] offset:1280\n\t"
    "v_cvt_pk_bf16_f32 v7, v53, v52\n\t"
    "ds_write_b32 v26, v5 offset:68\n\t"
    "v_cvt_pk_bf16_f32 v8, v54, v53\n\t"
    "ds_write_b16 v26, v4 offset:66\n\t"
    "v_cvt_pk_bf16_f32 v15, v58, v58\n\t"
    "ds_write_b16 v26, v5 offset:72\n\t"
    "v_and_b32 v14, 0xffff0000, v15\n\t"
    "ds_write_b64 v26, v[6:7] offset:576\n\t"
    "ds_write_b32 v26, v8 offset:1792\n\t"
    "ds_write_b16 v26, v6 offset:1790\n\t"
    "ds_write_b16_d16_hi v26, v7 offset:1796\n\t"
    "s_mov_b64 exec, 1\n\t"
    "ds_write_b64 v29, v[14:15] offset:56\n\t"
    "ds_write_b16 v29, v15 offset:64\n\t"
    "ds_write_b32 v29, v15 offset:1272\n\t"
    "ds_write_b32 v29, v15 offset:1276\n\t"
    "s_mov_b64 exec, -1\n\t"
    "s_waitcnt lgkmcnt(12)\n\t"
    "v_pk_add_f32 v[60:61], v[60:61], v[128:129]\n\t"
    "v_pk_add_f32 v[62:63], v[62:63], v[130:131]\n\t"
    "v_add_u32 v20, 16, v10\n\t"
    "v_cvt_pk_bf16_f32 v32, v58, v59\n\t"
    "v_cvt_pk_bf16_f32 v33, v60, v61\n\t"
    "v_cvt_pk_bf16_f32 v34, v60, v60\n\t"
    "v_cvt_pk_bf16_f32 v35, v62, v62\n\t"
    "ds_read2_b32 v[96:97], v21 offset0:144 offset1:145\n\t"
    "ds_read2_b32 v[98:99], v21 offset0:146 offset1:147\n\t"
    "ds_read2_b32 v[128:129], v21 offset0:16 offset1:17\n\t"
    "ds_read2_b32 v[130:131], v21 offset0:18 offset1:19\n\t"
    "ds_read2_b32 v[92:93], v21 offset0:160 offset1:161\n\t"
    "ds_read2_b32 v[94:95], v21 offset0:162 offset1:163\n\t"
    "ds_read2_b32 v[124:125], v21 offset0:32 offset1:33\n\t"
    "ds_read2_b32 v[126:127], v21 offset0:34 offset1:35\n\t"
    "ds_read2_b32 v[88:89], v21 offset0:176 offset1:177\n\t"
    "ds_read2_b32 v[90:91], v21 offset0:178 offset1:179\n\t"
    "ds_read2_b32 v[120:121], v21 offset0:48 offset1:49\n\t"
    "ds_read2_b32 v[122:123], v21 offset0:50 offset1:51\n\t"
    "ds_read2_b32 v[84:85], v21 offset0:192 offset1:193\n\t"
    "ds_read2_b32 v[86:87], v21 offset0:194 offset1:195\n\t"
    "ds_read2_b32 v[116:117], v21 offset0:64 offset1:65\n\t"
    "ds_read2_b32 v[118:119], v21 offset0:66 offset1:67\n\t"
    "ds_read2_b32 v[80:81], v21 offset0:208 offset1:209\n\t"
    "ds_read2_b32 v[82:83], v21 offset0:210 offset1:211\n\t"
    "ds_read2_b32 v[112:113], v21 offset0:80 offset1:81\n\t"
    "ds_read2_b32 v[114:115], v21 offset0:82 offset1:83\n\t"
    "s_waitcnt vmcnt(0)\n\t"
    "ds_read_b128 v[4:7], v24 offset:0\n\t"
    "ds_read_b128 v[8:11], v24 offset:64\n\t"
    "ds_read_b128 v[12:15], v24 offset:128\n\t"
    "s_waitcnt lgkmcnt(2)\n\t"
    "v_mfma_f32_16x16x32_bf16 v[52:55], v[96:99], v[4:7], 0\n\t"
    "ds_read_b128 v[16:19], v24 offset:192\n\t"
    "v_mfma_f32_16x16x32_bf16 v[56:59], v[128:131], v[4:7], 0\n\t"
    "s_waitcnt lgkmcnt(2)\n\t"
    "v_mfma_f32_16x16x32_bf16 v[52:55], v[92:95], v[8:11], v[52:55]\n\t"
    "ds_read_b128 v[244:247], v24 offset:256\n\t"
    "v_mfma_f32_16x16x32_bf16 v[56:59], v[124:127], v[8:11], v[56:59]\n\t"
    "s_waitcnt lgkmcnt(2)\n\t"
    "v_mfma_f32_16x16x32_bf16 v[52:55], v[88:91], v[12:15], v[52:55]\n\t"
    "ds_read2_b32 v[76:77], v21 offset0:224 offset1:225\n\t"
    "ds_read2_b32 v[78:79], v21 offset0:226 offset1:227\n\t"
    "v_mfma_f32_16x16x32_bf16 v[56:59], v[120:123], v[12:15], v[56:59]\n\t"
    "ds_read_b128 v[248:251], v24 offset:320\n\t"
    "ds_read2_b32 v[108:109], v21 offset0:96 offset1:97\n\t"
    "s_waitcnt lgkmcnt(5)\n\t"
    "v_mfma_f32_16x16x32_bf16 v[52:55], v[84:87], v[16:19], v[52:55]\n\t"
    "ds_read2_b32 v[110:111], v21 offset0:98 offset1:99\n\t"
    "ds_read2_b32 v[72:73], v21 offset0:240 offset1:241\n\t"
    "v_mfma_f32_16x16x32_bf16 v[56:59], v[116:119], v[16:19], v[56:59]\n\t"
    "ds_read2_b32 v[74:75], v21 offset0:242 offset1:243\n\t"
    "ds_read_b128 v[4:7], v24 offset:384\n\t"
    "s_waitcnt lgkmcnt(8)\n\t"
    "v_mfma_f32_16x16x32_bf16 v[52:55], v[80:83], v[244:247], v[52:55]\n\t"
    "ds_read2_b32 v[104:105], v21 offset0:112 offset1:113\n\t"
    "ds_read2_b32 v[106:107], v21 offset0:114 offset1:115\n\t"
    "v_mfma_f32_16x16x32_bf16 v[56:59], v[112:115], v[244:247], v[56:59]\n\t"
    "ds_read2_b32 v[68:69], v22 offset0:0 offset1:1\n\t"
    "ds_read2_b32 v[70:71], v22 offset0:2 offset1:3\n\t"
    "s_waitcnt lgkmcnt(9)\n\t"
    "v_mfma_f32_16x16x32_bf16 v[52:55], v[76:79], v[248:251], v[52:55]\n\t"
    "ds_read_b128 v[8:11], v24 offset:448\n\t"
    "ds_read2_b32 v[100:101], v21 offset0:128 offset1:129\n\t"
    "s_waitcnt lgkmcnt(9)\n\t"
    "v_mfma_f32_16x16x32_bf16 v[56:59], v[108:111], v[248:251], v[56:59]\n\t"
    "ds_read2_b32 v[102:103], v21 offset0:130 offset1:131\n\t"
    "ds_read2_b32 v[132:133], v21 offset0:0 offset1:1\n\t"
    "s_waitcnt lgkmcnt(8)\n\t"
    "v_mfma_f32_16x16x32_bf16 v[52:55], v[72:75], v[4:7], v[52:55]\n\t"
    "ds_read2_b32 v[134:135], v21 offset0:2 offset1:3\n\t"
    "ds_read_b128 v[12:15], v24 offset:512\n\t"
    "s_waitcnt lgkmcnt(8)\n\t"
    "v_mfma_f32_16x16x32_bf16 v[56:59], v[104:107], v[4:7], v[56:59]\n\t"
    "ds_read_b128 v[16:19], v24 offset:448\n\t"
    "s_waitcnt lgkmcnt(6)\n\t"
    "v_mfma_f32_16x16x32_bf16 v[52:55], v[68:71], v[8:11], v[52:55]\n\t"
    "ds_read_b128 v[244:247], v24 offset:576\n\t"
    "s_waitcnt lgkmcnt(5)\n\t"
    "v_mfma_f32_16x16x32_bf16 v[56:59], v[100:103], v[8:11], v[56:59]\n\t"
    "s_waitcnt lgkmcnt(3)\n\t"
    "v_mfma_f32_16x16x32_bf16 v[60:63], v[132:135], v[8:11], 0\n\t"
    "s_mov_b64 exec, -2\n\t"
    "ds_write_b16 v27, v32 offset:32\n\t"
    "ds_write_b16 v27, v32 offset:862\n\t"
    "ds_write_b16 v28, v33 offset:32\n\t"
    "ds_write_b16 v28, v33 offset:862\n\t"
    "s_mov_b64 exec, -1\n\t"
    "s_waitcnt lgkmcnt(6)\n\t"
    "v_mfma_f32_16x16x32_bf16 v[56:59], v[96:99], v[12:15], v[56:59]\n\t"
    "ds_read_b128 v[248:251], v24 offset:640\n\t"
    "v_cvt_pk_bf16_f32 v30, v52, v53\n\t"
    "v_mfma_f32_16x16x32_bf16 v[60:63], v[128:131], v[12:15], v[60:63]\n\t"
    "v_cvt_pk_bf16_f32 v31, v54, v55\n\t"
    "v_mov_b32 v3, 1\n\t"
    "s_waitcnt lgkmcnt(6)\n\t"
    "v_mfma_f32_16x16x32_bf16 v[168:171], v[16:19], v[36:39], 0\n\t"
    "ds_read_b128 v[4:7], v24 offset:704\n\t"
    "ds_write_b64 v25, v[30:31] offset:0\n\t"
    "s_waitcnt lgkmcnt(7)\n\t"
    "v_mfma_f32_16x16x32_bf16 v[56:59], v[92:95], v[244:247], v[56:59]\n\t"
    "ds_write_b32 v20, v3\n\t"
    "ds_write_b16_d16_hi v27, v32 offset:0\n\t"
    "v_mfma_f32_16x16x32_bf16 v[60:63], v[124:127], v[244:247], v[60:63]\n\t"
    "ds_write_b16_d16_hi v27, v32 offset:830\n\t"
    "v_mfma_f32_16x16x32_bf16 v[172:175], v[16:19], v[40:43], 0\n\t"
    "ds_read_b128 v[8:11], v24 offset:768\n\t"
    "ds_write_b16_d16_hi v28, v33 offset:0\n\t"
    "s_waitcnt lgkmcnt(7)\n\t"
    "v_mfma_f32_16x16x32_bf16 v[56:59], v[88:91], v[248:251], v[56:59]\n\t"
    "ds_read_b128 v[12:15], v24 offset:432\n\t"
    "v_mfma_f32_16x16x32_bf16 v[60:63], v[120:123], v[248:251], v[60:63]\n\t"
    "v_cvt_pk_bf16_f32 v136, v168, v169\n\t"
    "v_cvt_pk_bf16_f32 v137, v170, v171\n\t"
    "s_waitcnt lgkmcnt(7)\n\t"
    "v_mfma_f32_16x16x32_bf16 v[56:59], v[84:87], v[4:7], v[56:59]\n\t"
    "ds_read_b128 v[244:247], v24 offset:832\n\t"
    "ds_write_b16_d16_hi v28, v33 offset:830\n\t"
    "v_mfma_f32_16x16x32_bf16 v[176:179], v[16:19], v[44:47], 0\n\t"
    "s_mov_b64 exec, 1\n\t"
    "ds_write_b16 v29, v34 offset:2464\n\t"
    "ds_write_b16 v29, v34 offset:3294\n\t"
    "ds_write_b16 v29, v34 offset:1174\n\t"
    "s_mov_b64 exec, -1\n\t"
    "v_mfma_f32_16x16x32_bf16 v[60:63], v[116:119], v[4:7], v[60:63]\n\t"
    "v_cvt_pk_bf16_f32 v138, v172, v173\n\t"
    "v_cvt_pk_bf16_f32 v139, v174, v175\n\t"
    "s_waitcnt lgkmcnt(7)\n\t"
    "v_mfma_f32_16x16x32_bf16 v[56:59], v[80:83], v[8:11], v[56:59]\n\t"
    "ds_read_b128 v[248:251], v24 offset:896\n\t"
    "s_mov_b64 exec, 1\n\t"
    "ds_write_b16 v29, v34 offset:1176\n\t"
    "ds_write_b32 v29, v34 offset:2388\n\t"
    "ds_write_b16 v29, v35 offset:2944\n\t"
    "s_mov_b64 exec, -1\n\t"
    "s_waitcnt lgkmcnt(9)\n\t"
    "v_mfma_f32_16x16x32_bf16 v[180:183], v[12:15], v[36:39], 0\n\t"
    "v_mfma_f32_16x16x32_bf16 v[60:63], v[112:115], v[8:11], v[60:63]\n\t"
    "s_waitcnt lgkmcnt(8)\n\t"
    "v_mfma_f32_16x16x32_bf16 v[56:59], v[76:79], v[244:247], v[56:59]\n\t"
    "ds_read_b128 v[16:19], v24 offset:960\n\t"
    "v_cvt_pk_bf16_f32 v184, v176, v177\n\t"
    "v_mfma_f32_16x16x32_bf16 v[60:63], v[108:111], v[244:247], v[60:63]\n\t"
    "v_cvt_pk_bf16_f32 v185, v178, v179\n\t"
    "v_mfma_f32_16x16x32_bf16 v[168:171], v[12:15], v[40:43], 0\n\t"
    "s_waitcnt lgkmcnt(4)\n\t"
    "v_mfma_f32_16x16x32_bf16 v[56:59], v[72:75], v[248:251], v[56:59]\n\t"
    "ds_read_b128 v[4:7], v24 offset:1024\n\t"
    "v_cvt_pk_bf16_f32 v140, v180, v181\n\t"
    "v_mfma_f32_16x16x32_bf16 v[60:63], v[104:107], v[248:251], v[60:63]\n\t"
    "v_cvt_pk_bf16_f32 v141, v182, v183\n\t"
    "v_mfma_f32_16x16x32_bf16 v[172:175], v[12:15], v[48:51], 0\n\t"
    "ds_read_b128 v[8:11], v24 offset:960\n\t"
    "s_waitcnt lgkmcnt(2)\n\t"
    "v_mfma_f32_16x16x32_bf16 v[56:59], v[68:71], v[16:19], v[56:59]\n\t"
    "ds_read_b128 v[244:247], v24 offset:1088\n\t"
    "v_mfma_f32_16x16x32_bf16 v[60:63], v[100:103], v[16:19], v[60:63]\n\t"
    "v_cvt_pk_bf16_f32 v142, v168, v169\n\t"
    "v_cvt_pk_bf16_f32 v143, v170, v171\n\t"
    "v_mfma_f32_16x16x32_bf16 v[64:67], v[132:135], v[16:19], 0\n\t"
    "s_waitcnt lgkmcnt(2)\n\t"
    "v_mfma_f32_16x16x32_bf16 v[60:63], v[96:99], v[4:7], v[60:63]\n\t"
    "ds_read_b128 v[248:251], v24 offset:1152\n\t"
    "v_cvt_pk_bf16_f32 v30, v56, v57\n\t"
    "v_mfma_f32_16x16x32_bf16 v[64:67], v[128:131], v[4:7], v[64:67]\n\t"
    "v_cvt_pk_bf16_f32 v31, v58, v59\n\t"
    "v_mov_b32 v3, 2\n\t"
    "ds_write_b64 v25, v[30:31] offset:512\n\t"
    "ds_write_b32 v20, v3\n\t"
    "v_cvt_pk_bf16_f32 v186, v172, v173\n\t"
    "v_cvt_pk_bf16_f32 v187, v174, v175\n\t"
    "s_waitcnt lgkmcnt(4)\n\t"
    "v_mfma_f32_16x16x32_bf16 v[168:171], v[8:11], v[36:39], 0\n\t"
    "ds_read2_b32 v[200:201], v23 offset0:48 offset1:49\n\t"
    "ds_read2_b32 v[202:203], v23 offset0:80 offset1:81\n\t"
    "s_waitcnt lgkmcnt(5)\n\t"
    "v_mfma_f32_16x16x32_bf16 v[60:63], v[92:95], v[244:247], v[60:63]\n\t"
    "v_mfma_f32_16x16x32_bf16 v[64:67], v[124:127], v[244:247], v[64:67]\n\t"
    "ds_read_b128 v[12:15], v24 offset:1216\n\t"
    "v_mfma_f32_16x16x32_bf16 v[172:175], v[8:11], v[40:43], 0\n\t"
    "s_waitcnt lgkmcnt(5)\n\t"
    "v_mfma_f32_16x16x32_bf16 v[60:63], v[88:91], v[248:251], v[60:63]\n\t"
    "ds_read_b128 v[16:19], v24 offset:1280\n\t"
    "v_mfma_f32_16x16x32_bf16 v[64:67], v[120:123], v[248:251], v[64:67]\n\t"
    "ds_read_b128 v[4:7], v24 offset:944\n\t"
    "v_cvt_pk_bf16_f32 v144, v168, v169\n\t"
    "s_waitcnt lgkmcnt(3)\n\t"
    "v_mfma_f32_16x16x32_bf16 v[60:63], v[136:139], v[200:203], v[60:63]\n\t"
    "v_cvt_pk_bf16_f32 v145, v170, v171\n\t"
    "v_mfma_f32_16x16x32_bf16 v[176:179], v[8:11], v[44:47], 0\n\t"
    "ds_read_b128 v[244:247], v24 offset:1344\n\t"
    "s_waitcnt lgkmcnt(3)\n\t"
    "v_mfma_f32_16x16x32_bf16 v[60:63], v[84:87], v[12:15], v[60:63]\n\t"
    "v_cvt_pk_bf16_f32 v146, v172, v173\n\t"
    "v_cvt_pk_bf16_f32 v147, v174, v175\n\t"
    "v_mfma_f32_16x16x32_bf16 v[64:67], v[116:119], v[12:15], v[64:67]\n\t"
    "s_waitcnt lgkmcnt(2)\n\t"
    "v_mfma_f32_16x16x32_bf16 v[60:63], v[80:83], v[16:19], v[60:63]\n\t"
    "ds_read_b128 v[248:251], v24 offset:1408\n\t"
    "s_waitcnt lgkmcnt(2)\n\t"
    "v_mfma_f32_16x16x32_bf16 v[180:183], v[4:7], v[36:39], 0\n\t"
    "v_mfma_f32_16x16x32_bf16 v[64:67], v[112:115], v[16:19], v[64:67]\n\t"
    "ds_read2_b32 v[204:205], v23 offset0:112 offset1:113\n\t"
    "ds_read2_b32 v[206:207], v23 offset0:16 offset1:17\n\t"
    "s_waitcnt lgkmcnt(3)\n\t"
    "v_mfma_f32_16x16x32_bf16 v[60:63], v[76:79], v[244:247], v[60:63]\n\t"
    "v_cvt_pk_bf16_f32 v188, v176, v177\n\t"
    "v_cvt_pk_bf16_f32 v189, v178, v179\n\t"
    "v_mfma_f32_16x16x32_bf16 v[64:67], v[108:111], v[244:247], v[64:67]\n\t"
    "ds_read_b128 v[8:11], v24 offset:1472\n\t"
    "v_mfma_f32_16x16x32_bf16 v[168:171], v[4:7], v[40:43], 0\n\t"
    "s_waitcnt lgkmcnt(3)\n\t"
    "v_mfma_f32_16x16x32_bf16 v[60:63], v[72:75], v[248:251], v[60:63]\n\t"
    "v_cvt_pk_bf16_f32 v148, v180, v181\n\t"
    "v_cvt_pk_bf16_f32 v149, v182, v183\n\t"
    "v_mfma_f32_16x16x32_bf16 v[64:67], v[104:107], v[248:251], v[64:67]\n\t"
    "ds_read_b128 v[12:15], v24 offset:1536\n\t"
    "s_waitcnt lgkmcnt(2)\n\t"
    "v_mfma_f32_16x16x32_bf16 v[60:63], v[140:143], v[204:207], v[60:63]\n\t"
    "v_mfma_f32_16x16x32_bf16 v[172:175], v[4:7], v[48:51], 0\n\t"
    "ds_read_b128 v[16:19], v24 offset:1472\n\t"
    "s_waitcnt lgkmcnt(2)\n\t"
    "v_mfma_f32_16x16x32_bf16 v[60:63], v[68:71], v[8:11], v[60:63]\n\t"
    "ds_read2_b32 v[208:209], v23 offset0:40 offset1:41\n\t"
    "ds_read2_b32 v[210:211], v23 offset0:72 offset1:73\n\t"
    "v_mfma_f32_16x16x32_bf16 v[64:67], v[100:103], v[8:11], v[64:67]\n\t"
    "ds_read_b128 v[244:247], v24 offset:1600\n\t"
    "v_cvt_pk_bf16_f32 v150, v168, v169\n\t"
    "v_mfma_f32_16x16x32_bf16 v[52:55], v[132:135], v[8:11], 0\n\t"
    "v_cvt_pk_bf16_f32 v151, v170, v171\n\t"
    "s_waitcnt lgkmcnt(4)\n\t"
    "v_mfma_f32_16x16x32_bf16 v[64:67], v[96:99], v[12:15], v[64:67]\n\t"
    "v_cvt_pk_bf16_f32 v30, v60, v61\n\t"
    "v_cvt_pk_bf16_f32 v31, v62, v63\n\t"
    "v_mfma_f32_16x16x32_bf16 v[52:55], v[128:131], v[12:15], v[52:55]\n\t"
    "ds_read_b128 v[248:251], v24 offset:1664\n\t"
    "v_mov_b32 v3, 3\n\t"
    "ds_write_b64 v25, v[30:31] offset:1024\n\t"
    "ds_write_b32 v20, v3\n\t"
    "v_cvt_pk_bf16_f32 v190, v172, v173\n\t"
    "v_cvt_pk_bf16_f32 v191, v174, v175\n\t"
    "s_waitcnt lgkmcnt(6)\n\t"
    "v_mfma_f32_16x16x32_bf16 v[168:171], v[16:19], v[36:39], 0\n\t"
    "s_waitcnt lgkmcnt(4)\n\t"
    "v_mfma_f32_16x16x32_bf16 v[64:67], v[136:139], v[208:211], v[64:67]\n\t"
    "ds_read2_b32 v[212:213], v23 offset0:104 offset1:105\n\t"
    "ds_read2_b32 v[214:215], v23 offset0:8 offset1:9\n\t"
    "s_waitcnt lgkmcnt(5)\n\t"
    "v_mfma_f32_16x16x32_bf16 v[64:67], v[92:95], v[244:247], v[64:67]\n\t"
    "v_mfma_f32_16x16x32_bf16 v[52:55], v[124:127], v[244:247], v[52:55]\n\t"
    "ds_read_b128 v[4:7], v24 offset:1728\n\t"
    "v_mfma_f32_16x16x32_bf16 v[172:175], v[16:19], v[40:43], 0\n\t"
    "s_waitcnt lgkmcnt(5)\n\t"
    "v_mfma_f32_16x16x32_bf16 v[64:67], v[88:91], v[248:251], v[64:67]\n\t"
    "ds_read_b128 v[8:11], v24 offset:1792\n\t"
    "v_cvt_pk_bf16_f32 v152, v168, v169\n\t"
    "v_mfma_f32_16x16x32_bf16 v[52:55], v[120:123], v[248:251], v[52:55]\n\t"
    "ds_read_b128 v[12:15], v24 offset:1456\n\t"
    "v_cvt_pk_bf16_f32 v153, v170, v171\n\t"
    "s_waitcnt lgkmcnt(3)\n\t"
    "v_mfma_f32_16x16x32_bf16 v[64:67], v[140:143], v[212:215], v[64:67]\n\t"
    "v_mfma_f32_16x16x32_bf16 v[176:179], v[16:19], v[44:47], 0\n\t"
    "s_waitcnt lgkmcnt(2)\n\t"
    "v_mfma_f32_16x16x32_bf16 v[64:67], v[84:87], v[4:7], v[64:67]\n\t"
    "ds_read_b128 v[244:247], v24 offset:1856\n\t"
    "v_cvt_pk_bf16_f32 v154, v172, v173\n\t"
    "v_mfma_f32_16x16x32_bf16 v[52:55], v[116:119], v[4:7], v[52:55]\n\t"
    "v_cvt_pk_bf16_f32 v155, v174, v175\n\t"
    "s_waitcnt lgkmcnt(2)\n\t"
    "v_mfma_f32_16x16x32_bf16 v[64:67], v[80:83], v[8:11], v[64:67]\n\t"
    "s_waitcnt lgkmcnt(1)\n\t"
    "v_mfma_f32_16x16x32_bf16 v[180:183], v[12:15], v[36:39], 0\n\t"
    "ds_read_b128 v[248:251], v24 offset:1920\n\t"
    "v_mfma_f32_16x16x32_bf16 v[52:55], v[112:115], v[8:11], v[52:55]\n\t"
    "v_cvt_pk_bf16_f32 v192, v176, v177\n\t"
    "v_cvt_pk_bf16_f32 v193, v178, v179\n\t"
    "v_mfma_f32_16x16x32_bf16 v[64:67], v[144:147], v[200:203], v[64:67]\n\t"
    "s_waitcnt lgkmcnt(1)\n\t"
    "v_mfma_f32_16x16x32_bf16 v[64:67], v[76:79], v[244:247], v[64:67]\n\t"
    "v_mfma_f32_16x16x32_bf16 v[168:171], v[12:15], v[40:43], 0\n\t"
    "ds_read_b128 v[16:19], v24 offset:1984\n\t"
    "v_mfma_f32_16x16x32_bf16 v[52:55], v[108:111], v[244:247], v[52:55]\n\t"
    "v_cvt_pk_bf16_f32 v156, v180, v181\n\t"
    "v_cvt_pk_bf16_f32 v157, v182, v183\n\t"
    "s_waitcnt lgkmcnt(1)\n\t"
    "v_mfma_f32_16x16x32_bf16 v[64:67], v[72:75], v[248:251], v[64:67]\n\t"
    "v_mfma_f32_16x16x32_bf16 v[52:55], v[104:107], v[248:251], v[52:55]\n\t"
    "ds_read2_b32 v[240:241], v22 offset0:24 offset1:25\n\t"
    "ds_read2_b32 v[242:243], v22 offset0:26 offset1:27\n\t"
    "v_mfma_f32_16x16x32_bf16 v[172:175], v[12:15], v[48:51], 0\n\t"
    "ds_read_b128 v[4:7], v24 offset:448\n\t"
    "ds_read_b128 v[8:11], v24 offset:2048\n\t"
    "v_mfma_f32_16x16x32_bf16 v[64:67], v[148:151], v[204:207], v[64:67]\n\t"
    "ds_read_b128 v[244:247], v24 offset:1984\n\t"
    "v_cvt_pk_bf16_f32 v158, v168, v169\n\t"
    "s_waitcnt lgkmcnt(5)\n\t"
    "v_mfma_f32_16x16x32_bf16 v[64:67], v[68:71], v[16:19], v[64:67]\n\t"
    "v_cvt_pk_bf16_f32 v159, v170, v171\n\t"
    "v_mfma_f32_16x16x32_bf16 v[52:55], v[100:103], v[16:19], v[52:55]\n\t"
    "ds_read2_b32 v[224:225], v23 offset0:192 offset1:193\n\t"
    "ds_read2_b32 v[226:227], v23 offset0:152 offset1:153\n\t"
    "v_mfma_f32_16x16x32_bf16 v[56:59], v[132:135], v[16:19], 0\n\t"
    "ds_read_b128 v[248:251], v24 offset:2112\n\t"
    "s_waitcnt lgkmcnt(5)\n\t"
    "v_mfma_f32_16x16x32_bf16 v[52:55], v[240:243], v[4:7], v[52:55]\n\t"
    "v_cvt_pk_bf16_f32 v194, v172, v173\n\t"
    "v_cvt_pk_bf16_f32 v195, v174, v175\n\t"
    "s_waitcnt lgkmcnt(4)\n\t"
    "v_mfma_f32_16x16x32_bf16 v[52:55], v[96:99], v[8:11], v[52:55]\n\t"
    "v_cvt_pk_bf16_f32 v30, v64, v65\n\t"
    "v_cvt_pk_bf16_f32 v31, v66, v67\n\t"
    "s_waitcnt lgkmcnt(3)\n\t"
    "v_mfma_f32_16x16x32_bf16 v[168:171], v[244:247], v[36:39], 0\n\t"
    "ds_read2_b32 v[216:217], v23 offset0:32 offset1:33\n\t"
    "ds_read2_b32 v[218:219], v23 offset0:64 offset1:65\n\t"
    "v_mfma_f32_16x16x32_bf16 v[56:59], v[128:131], v[8:11], v[56:59]\n\t"
    "ds_read_b128 v[12:15], v24 offset:2176\n\t"
    "v_mov_b32 v3, 4\n\t"
    "s_waitcnt lgkmcnt(4)\n\t"
    "v_mfma_f32_16x16x32_bf16 v[52:55], v[184:187], v[224:227], v[52:55]\n\t"
    "ds_write_b64 v25, v[30:31] offset:1536\n\t"
    "ds_write_b32 v20, v3\n\t"
    "s_waitcnt lgkmcnt(5)\n\t"
    "v_mfma_f32_16x16x32_bf16 v[52:55], v[92:95], v[248:251], v[52:55]\n\t"
    "v_mfma_f32_16x16x32_bf16 v[172:175], v[244:247], v[40:43], 0\n\t"
    "ds_read2_b32 v[220:221], v23 offset0:96 offset1:97\n\t"
    "ds_read2_b32 v[222:223], v23 offset0:0 offset1:1\n\t"
    "v_mfma_f32_16x16x32_bf16 v[56:59], v[124:127], v[248:251], v[56:59]\n\t"
    "ds_read_b128 v[16:19], v24 offset:2240\n\t"
    "v_cvt_pk_bf16_f32 v160, v168, v169\n\t"
    "s_waitcnt lgkmcnt(6)\n\t"
    "v_mfma_f32_16x16x32_bf16 v[52:55], v[136:139], v[216:219], v[52:55]\n\t"
    "v_cvt_pk_bf16_f32 v161, v170, v171\n\t"
    "s_waitcnt lgkmcnt(5)\n\t"
    "v_mfma_f32_16x16x32_bf16 v[52:55], v[88:91], v[12:15], v[52:55]\n\t"
    "v_mfma_f32_16x16x32_bf16 v[56:59], v[120:123], v[12:15], v[56:59]\n\t"
    "ds_read_b128 v[4:7], v24 offset:1968\n\t"
    "v_mfma_f32_16x16x32_bf16 v[176:179], v[244:247], v[44:47], 0\n\t"
    "ds_read_b128 v[8:11], v24 offset:2304\n\t"
    "v_cvt_pk_bf16_f32 v162, v172, v173\n\t"
    "s_waitcnt lgkmcnt(3)\n\t"
    "v_mfma_f32_16x16x32_bf16 v[52:55], v[140:143], v[220:223], v[52:55]\n\t"
    "v_cvt_pk_bf16_f32 v163, v174, v175\n\t"
    "s_waitcnt lgkmcnt(2)\n\t"
    "v_mfma_f32_16x16x32_bf16 v[52:55], v[84:87], v[16:19], v[52:55]\n\t"
    "v_mfma_f32_16x16x32_bf16 v[56:59], v[116:119], v[16:19], v[56:59]\n\t"
    "ds_read_b128 v[248:251], v24 offset:2368\n\t"
    "v_mfma_f32_16x16x32_bf16 v[52:55], v[144:147], v[208:211], v[52:55]\n\t"
    "s_waitcnt lgkmcnt(2)\n\t"
    "v_mfma_f32_16x16x32_bf16 v[180:183], v[4:7], v[36:39], 0\n\t"
    "v_cvt_pk_bf16_f32 v196, v176, v177\n\t"
    "v_cvt_pk_bf16_f32 v197, v178, v179\n\t"
    "s_waitcnt lgkmcnt(1)\n\t"
    "v_mfma_f32_16x16x32_bf16 v[52:55], v[80:83], v[8:11], v[52:55]\n\t"
    "v_mfma_f32_16x16x32_bf16 v[56:59], v[112:115], v[8:11], v[56:59]\n\t"
    "ds_read_b128 v[12:15], v24 offset:2432\n\t"
    "v_mfma_f32_16x16x32_bf16 v[52:55], v[148:151], v[212:215], v[52:55]\n\t"
    "s_waitcnt lgkmcnt(1)\n\t"
    "v_mfma_f32_16x16x32_bf16 v[52:55], v[76:79], v[248:251], v[52:55]\n\t"
    "v_mfma_f32_16x16x32_bf16 v[168:171], v[4:7], v[40:43], 0\n\t"
    "v_cvt_pk_bf16_f32 v164, v180, v181\n\t"
    "v_cvt_pk_bf16_f32 v165, v182, v183\n\t"
    "v_mfma_f32_16x16x32_bf16 v[56:59], v[108:111], v[248:251], v[56:59]\n\t"
    "ds_read_b128 v[244:247], v24 offset:2496\n\t"
    "v_mfma_f32_16x16x32_bf16 v[52:55], v[152:155], v[200:203], v[52:55]\n\t"
    "s_waitcnt lgkmcnt(1)\n\t"
    "v_mfma_f32_16x16x32_bf16 v[52:55], v[72:75], v[12:15], v[52:55]\n\t"
    "v_mfma_f32_16x16x32_bf16 v[56:59], v[104:107], v[12:15], v[56:59]\n\t"
    "ds_read_b128 v[16:19], v24 offset:960\n\t"
    "v_mfma_f32_16x16x32_bf16 v[172:175], v[4:7], v[48:51], 0\n\t"
    "ds_read_b128 v[8:11], v24 offset:2560\n\t"
    "v_cvt_pk_bf16_f32 v166, v168, v169\n\t"
    "v_mfma_f32_16x16x32_bf16 v[52:55], v[156:159], v[204:207], v[52:55]\n\t"
    "ds_read_b128 v[248:251], v24 offset:2496\n\t"
    "v_cvt_pk_bf16_f32 v167, v170, v171\n\t"
    "s_waitcnt lgkmcnt(3)\n\t"
    "v_mfma_f32_16x16x32_bf16 v[52:55], v[68:71], v[244:247], v[52:55]\n\t"
    "v_mfma_f32_16x16x32_bf16 v[56:59], v[100:103], v[244:247], v[56:59]\n\t"
    "ds_read2_b32 v[228:229], v23 offset0:184 offset1:185\n\t"
    "ds_read2_b32 v[230:231], v23 offset0:144 offset1:145\n\t"
    "v_mfma_f32_16x16x32_bf16 v[60:63], v[132:135], v[244:247], 0\n\t"
    "ds_read_b128 v[12:15], v24 offset:2624\n\t"
    "s_waitcnt lgkmcnt(5)\n\t"
    "v_mfma_f32_16x16x32_bf16 v[56:59], v[240:243], v[16:19], v[56:59]\n\t"
    "v_cvt_pk_bf16_f32 v198, v172, v173\n\t"
    "v_cvt_pk_bf16_f32 v199, v174, v175\n\t"
    "s_waitcnt lgkmcnt(4)\n\t"
    "v_mfma_f32_16x16x32_bf16 v[56:59], v[96:99], v[8:11], v[56:59]\n\t"
    "v_cvt_pk_bf16_f32 v30, v52, v53\n\t"
    "v_cvt_pk_bf16_f32 v31, v54, v55\n\t"
    "s_waitcnt lgkmcnt(3)\n\t"
    "v_mfma_f32_16x16x32_bf16 v[136:139], v[248:251], v[36:39], 0\n\t"
    "ds_read_b128 v[4:7], v24 offset:2688\n\t"
    "v_mov_b32 v3, 5\n\t"
    "v_mfma_f32_16x16x32_bf16 v[60:63], v[128:131], v[8:11], v[60:63]\n\t"
    "ds_write_b64 v25, v[30:31] offset:2048\n\t"
    "ds_write_b32 v20, v3\n\t"
    "s_waitcnt lgkmcnt(4)\n\t"
    "v_mfma_f32_16x16x32_bf16 v[56:59], v[184:187], v[228:231], v[56:59]\n\t"
    "s_waitcnt lgkmcnt(3)\n\t"
    "v_mfma_f32_16x16x32_bf16 v[56:59], v[92:95], v[12:15], v[56:59]\n\t"
    "v_mfma_f32_16x16x32_bf16 v[60:63], v[124:127], v[12:15], v[60:63]\n\t"
    "ds_read_b128 v[244:247], v24 offset:2752\n\t"
    "v_mfma_f32_16x16x32_bf16 v[56:59], v[188:191], v[224:227], v[56:59]\n\t"
    "v_cvt_pk_bf16_f32 v168, v136, v137\n\t"
    "v_cvt_pk_bf16_f32 v169, v138, v139\n\t"
    "s_waitcnt lgkmcnt(3)\n\t"
    "v_mfma_f32_16x16x32_bf16 v[56:59], v[88:91], v[4:7], v[56:59]\n\t"
    "v_mfma_f32_16x16x32_bf16 v[140:143], v[248:251], v[40:43], 0\n\t"
    "ds_read_b128 v[16:19], v24 offset:2816\n\t"
    "v_mfma_f32_16x16x32_bf16 v[60:63], v[120:123], v[4:7], v[60:63]\n\t"
    "ds_read_b128 v[8:11], v24 offset:2480\n\t"
    "v_mfma_f32_16x16x32_bf16 v[56:59], v[144:147], v[216:219], v[56:59]\n\t"
    "s_waitcnt lgkmcnt(2)\n\t"
    "v_mfma_f32_16x16x32_bf16 v[56:59], v[84:87], v[244:247], v[56:59]\n\t"
    "v_mfma_f32_16x16x32_bf16 v[60:63], v[116:119], v[244:247], v[60:63]\n\t"
    "ds_read_b128 v[12:15], v24 offset:2880\n\t"
    "v_mfma_f32_16x16x32_bf16 v[56:59], v[148:151], v[220:223], v[56:59]\n\t"
    "v_cvt_pk_bf16_f32 v170, v140, v141\n\t"
    "v_cvt_pk_bf16_f32 v171, v142, v143\n\t"
    "s_waitcnt lgkmcnt(2)\n\t"
    "v_mfma_f32_16x16x32_bf16 v[56:59], v[80:83], v[16:19], v[56:59]\n\t"
    "s_waitcnt lgkmcnt(1)\n\t"
    "v_mfma_f32_16x16x32_bf16 v[136:139], v[8:11], v[36:39], 0\n\t"
    "ds_read_b128 v[248:251], v24 offset:2944\n\t"
    "v_mfma_f32_16x16x32_bf16 v[60:63], v[112:115], v[16:19], v[60:63]\n\t"
    "v_mfma_f32_16x16x32_bf16 v[56:59], v[152:155], v[208:211], v[56:59]\n\t"
    "s_waitcnt lgkmcnt(1)\n\t"
    "v_mfma_f32_16x16x32_bf16 v[56:59], v[76:79], v[12:15], v[56:59]\n\t"
    "v_mfma_f32_16x16x32_bf16 v[60:63], v[108:111], v[12:15], v[60:63]\n\t"
    "v_mfma_f32_16x16x32_bf16 v[56:59], v[156:159], v[212:215], v[56:59]\n\t"
    "ds_read_b128 v[4:7], v24 offset:3008\n\t"
    "v_cvt_pk_bf16_f32 v172, v136, v137\n\t"
    "s_waitcnt lgkmcnt(1)\n\t"
    "v_mfma_f32_16x16x32_bf16 v[56:59], v[72:75], v[248:251], v[56:59]\n\t"
    "v_cvt_pk_bf16_f32 v173, v138, v139\n\t"
    "v_mfma_f32_16x16x32_bf16 v[140:143], v[8:11], v[40:43], 0\n\t"
    "v_mfma_f32_16x16x32_bf16 v[60:63], v[104:107], v[248:251], v[60:63]\n\t"
    "ds_read_b128 v[244:247], v24 offset:1472\n\t"
    "v_mfma_f32_16x16x32_bf16 v[56:59], v[160:163], v[200:203], v[56:59]\n\t"
    "ds_read_b128 v[16:19], v24 offset:3072\n\t"
    "v_mfma_f32_16x16x32_bf16 v[56:59], v[164:167], v[204:207], v[56:59]\n\t"
    "ds_read_b128 v[12:15], v24 offset:3008\n\t"
    "s_waitcnt lgkmcnt(3)\n\t"
    "v_mfma_f32_16x16x32_bf16 v[56:59], v[68:71], v[4:7], v[56:59]\n\t"
    "v_mfma_f32_16x16x32_bf16 v[60:63], v[100:103], v[4:7], v[60:63]\n\t"
    "ds_read2_b32 v[232:233], v23 offset0:176 offset1:177\n\t"
    "ds_read2_b32 v[234:235], v23 offset0:136 offset1:137\n\t"
    "v_mfma_f32_16x16x32_bf16 v[64:67], v[132:135], v[4:7], 0\n\t"
    "ds_read_b128 v[8:11], v24 offset:3136\n\t"
    "v_cvt_pk_bf16_f32 v174, v140, v141\n\t"
    "s_waitcnt lgkmcnt(5)\n\t"
    "v_mfma_f32_16x16x32_bf16 v[60:63], v[240:243], v[244:247], v[60:63]\n\t"
    "v_cvt_pk_bf16_f32 v175, v142, v143\n\t"
    "v_cvt_pk_bf16_f32 v30, v56, v57\n\t"
    "s_waitcnt lgkmcnt(4)\n\t"
    "v_mfma_f32_16x16x32_bf16 v[60:63], v[96:99], v[16:19], v[60:63]\n\t"
    "v_cvt_pk_bf16_f32 v31, v58, v59\n\t"
    "v_mov_b32 v3, 6\n\t"
    "s_waitcnt lgkmcnt(3)\n\t"
    "v_mfma_f32_16x16x32_bf16 v[136:139], v[12:15], v[36:39], 0\n\t"
    "ds_read_b128 v[248:251], v24 offset:3200\n\t"
    "ds_write_b64 v25, v[30:31] offset:2560\n\t"
    "v_mfma_f32_16x16x32_bf16 v[64:67], v[128:131], v[16:19], v[64:67]\n\t"
    "ds_write_b32 v20, v3\n\t"
    "s_waitcnt lgkmcnt(4)\n\t"
    "v_mfma_f32_16x16x32_bf16 v[60:63], v[184:187], v[232:235], v[60:63]\n\t"
    "s_waitcnt lgkmcnt(3)\n\t"
    "v_mfma_f32_16x16x32_bf16 v[60:63], v[92:95], v[8:11], v[60:63]\n\t"
    "v_mfma_f32_16x16x32_bf16 v[64:67], v[124:127], v[8:11], v[64:67]\n\t"
    "v_mfma_f32_16x16x32_bf16 v[60:63], v[188:191], v[228:231], v[60:63]\n\t"
    "ds_read_b128 v[4:7], v24 offset:3264\n\t"
    "v_cvt_pk_bf16_f32 v176, v136, v137\n\t"
    "s_waitcnt lgkmcnt(3)\n\t"
    "v_mfma_f32_16x16x32_bf16 v[60:63], v[88:91], v[248:251], v[60:63]\n\t"
    "v_cvt_pk_bf16_f32 v177, v138, v139\n\t"
    "v_mfma_f32_16x16x32_bf16 v[140:143], v[12:15], v[40:43], 0\n\t"
    "v_mfma_f32_16x16x32_bf16 v[64:67], v[120:123], v[248:251], v[64:67]\n\t"
    "ds_read_b128 v[244:247], v24 offset:2992\n\t"
    "v_mfma_f32_16x16x32_bf16 v[60:63], v[192:195], v[224:227], v[60:63]\n\t"
    "ds_read_b128 v[16:19], v24 offset:3328\n\t"
    "v_mfma_f32_16x16x32_bf16 v[60:63], v[152:155], v[216:219], v[60:63]\n\t"
    "s_waitcnt lgkmcnt(2)\n\t"
    "v_mfma_f32_16x16x32_bf16 v[60:63], v[84:87], v[4:7], v[60:63]\n\t"
    "v_mfma_f32_16x16x32_bf16 v[64:67], v[116:119], v[4:7], v[64:67]\n\t"
    "ds_read_b128 v[8:11], v24 offset:3392\n\t"
    "v_cvt_pk_bf16_f32 v178, v140, v141\n\t"
    "v_mfma_f32_16x16x32_bf16 v[60:63], v[156:159], v[220:223], v[60:63]\n\t"
    "v_cvt_pk_bf16_f32 v179, v142, v143\n\t"
    "s_waitcnt lgkmcnt(2)\n\t"
    "v_mfma_f32_16x16x32_bf16 v[136:139], v[244:247], v[36:39], 0\n\t"
    "s_waitcnt lgkmcnt(1)\n\t"
    "v_mfma_f32_16x16x32_bf16 v[60:63], v[80:83], v[16:19], v[60:63]\n\t"
    "v_mfma_f32_16x16x32_bf16 v[64:67], v[112:115], v[16:19], v[64:67]\n\t"
    "ds_read_b128 v[12:15], v24 offset:3456\n\t"
    "v_mfma_f32_16x16x32_bf16 v[60:63], v[160:163], v[208:211], v[60:63]\n\t"
    "s_waitcnt lgkmcnt(1)\n\t"
    "v_mfma_f32_16x16x32_bf16 v[60:63], v[76:79], v[8:11], v[60:63]\n\t"
    "v_mfma_f32_16x16x32_bf16 v[64:67], v[108:111], v[8:11], v[64:67]\n\t"
    "v_cvt_pk_bf16_f32 v180, v136, v137\n\t"
    "v_cvt_pk_bf16_f32 v181, v138, v139\n\t"
    "v_mfma_f32_16x16x32_bf16 v[60:63], v[164:167], v[212:215], v[60:63]\n\t"
    "ds_read_b128 v[248:251], v24 offset:3520\n\t"
    "v_mfma_f32_16x16x32_bf16 v[140:143], v[244:247], v[40:43], 0\n\t"
    "s_waitcnt lgkmcnt(1)\n\t"
    "v_mfma_f32_16x16x32_bf16 v[60:63], v[72:75], v[12:15], v[60:63]\n\t"
    "ds_read_b128 v[4:7], v24 offset:1984\n\t"
    "v_mfma_f32_16x16x32_bf16 v[64:67], v[104:107], v[12:15], v[64:67]\n\t"
    "ds_read_b128 v[16:19], v24 offset:3584\n\t"
    "v_mfma_f32_16x16x32_bf16 v[60:63], v[168:171], v[200:203], v[60:63]\n\t"
    "ds_read2_b32 v[236:237], v23 offset0:168 offset1:169\n\t"
    "ds_read2_b32 v[238:239], v23 offset0:128 offset1:129\n\t"
    "v_mfma_f32_16x16x32_bf16 v[60:63], v[172:175], v[204:207], v[60:63]\n\t"
    "ds_read_b128 v[8:11], v24 offset:3648\n\t"
    "s_waitcnt lgkmcnt(5)\n\t"
    "v_mfma_f32_16x16x32_bf16 v[60:63], v[68:71], v[248:251], v[60:63]\n\t"
    "v_cvt_pk_bf16_f32 v182, v140, v141\n\t"
    "v_cvt_pk_bf16_f32 v183, v142, v143\n\t"
    "v_mfma_f32_16x16x32_bf16 v[64:67], v[100:103], v[248:251], v[64:67]\n\t"
    "s_waitcnt lgkmcnt(4)\n\t"
    "v_mfma_f32_16x16x32_bf16 v[64:67], v[240:243], v[4:7], v[64:67]\n\t"
    "ds_read_b128 v[244:247], v24 offset:3712\n\t"
    "s_waitcnt lgkmcnt(4)\n\t"
    "v_mfma_f32_16x16x32_bf16 v[64:67], v[96:99], v[16:19], v[64:67]\n\t"
    "v_cvt_pk_bf16_f32 v30, v60, v61\n\t"
    "v_cvt_pk_bf16_f32 v31, v62, v63\n\t"
    "s_waitcnt lgkmcnt(2)\n\t"
    "v_mfma_f32_16x16x32_bf16 v[64:67], v[184:187], v[236:239], v[64:67]\n\t"
    "ds_read_b128 v[12:15], v24 offset:3776\n\t"
    "v_mov_b32 v3, 7\n\t"
    "s_waitcnt lgkmcnt(2)\n\t"
    "v_mfma_f32_16x16x32_bf16 v[64:67], v[92:95], v[8:11], v[64:67]\n\t"
    "ds_write_b64 v25, v[30:31] offset:3072\n\t"
    "ds_write_b32 v20, v3\n\t"
    "v_mfma_f32_16x16x32_bf16 v[64:67], v[188:191], v[232:235], v[64:67]\n\t"
    "ds_read_b128 v[248:251], v24 offset:3840\n\t"
    "v_mfma_f32_16x16x32_bf16 v[64:67], v[192:195], v[228:231], v[64:67]\n\t"
    "s_waitcnt lgkmcnt(4)\n\t"
    "v_mfma_f32_16x16x32_bf16 v[64:67], v[88:91], v[244:247], v[64:67]\n\t"
    "v_mfma_f32_16x16x32_bf16 v[64:67], v[196:199], v[224:227], v[64:67]\n\t"
    "ds_read_b128 v[4:7], v24 offset:3904\n\t"
    "s_waitcnt lgkmcnt(4)\n\t"
    "v_mfma_f32_16x16x32_bf16 v[64:67], v[84:87], v[12:15], v[64:67]\n\t"
    "v_mfma_f32_16x16x32_bf16 v[64:67], v[160:163], v[216:219], v[64:67]\n\t"
    "ds_read_b128 v[16:19], v24 offset:3968\n\t"
    "s_waitcnt lgkmcnt(2)\n\t"
    "v_mfma_f32_16x16x32_bf16 v[64:67], v[80:83], v[248:251], v[64:67]\n\t"
    "v_mfma_f32_16x16x32_bf16 v[64:67], v[164:167], v[220:223], v[64:67]\n\t"
    "v_mfma_f32_16x16x32_bf16 v[64:67], v[168:171], v[208:211], v[64:67]\n\t"
    "ds_read_b128 v[8:11], v24 offset:4032\n\t"
    "s_waitcnt lgkmcnt(2)\n\t"
    "v_mfma_f32_16x16x32_bf16 v[64:67], v[76:79], v[4:7], v[64:67]\n\t"
    "v_mfma_f32_16x16x32_bf16 v[64:67], v[172:175], v[212:215], v[64:67]\n\t"
    "s_waitcnt lgkmcnt(1)\n\t"
    "v_mfma_f32_16x16x32_bf16 v[64:67], v[72:75], v[16:19], v[64:67]\n\t"
    "v_mfma_f32_16x16x32_bf16 v[64:67], v[176:179], v[200:203], v[64:67]\n\t"
    "v_mfma_f32_16x16x32_bf16 v[64:67], v[180:183], v[204:207], v[64:67]\n\t"
    "s_waitcnt lgkmcnt(0)\n\t"
    "v_mfma_f32_16x16x32_bf16 v[64:67], v[68:71], v[8:11], v[64:67]\n\t"
    "s_nop 7\n\t"
    "v_cvt_pk_bf16_f32 v30, v64, v65\n\t"
    "v_cvt_pk_bf16_f32 v31, v66, v67\n\t"
    "v_mov_b32 v3, 8\n\t"
    "ds_write_b64 v25, v[30:31] offset:3584\n\t"
    "ds_write_b32 v20, v3\n\t"
    "s_endpgm\n\t"
    ".Lur_%=:\n\t"
    ".Lur61_%=0:\n\t"
    "s_waitcnt vmcnt(0)\n\t"
    "ds_read_b64_tr_b16 v[36:37], v32 offset:0\n\t"
    "ds_read_b64_tr_b16 v[38:39], v32 offset:512\n\t"
    "ds_read_b64_tr_b16 v[40:41], v33 offset:0\n\t"
    "ds_read_b64_tr_b16 v[42:43], v33 offset:512\n\t"
    "ds_read_b64_tr_b16 v[44:45], v34 offset:0\n\t"
    "ds_read_b64_tr_b16 v[46:47], v34 offset:512\n\t"
    "ds_read_b64_tr_b16 v[48:49], v35 offset:0\n\t"
    "ds_read_b64_tr_b16 v[50:51], v35 offset:512\n\t"
    "v_mov_b64 v[32:33], 0\n\t"
    "v_mov_b64 v[34:35], 0\n\t"
    "ds_write_b128 v31, v[32:35] offset:0\n\t"
    "ds_write_b128 v31, v[32:35] offset:1024\n\t"
    "ds_write_b128 v31, v[32:35] offset:2048\n\t"
    "ds_write_b128 v31, v[32:35] offset:3072\n\t"
    "v_lshl_add_u32 v31, v1, 3, s18\n\t"
    "v_and_b32 v249, 60, v1\n\t"
    "v_and_b32 v248, 3, v1\n\t"
    "v_lshlrev_b32 v249, 6, v249\n\t"
    "v_lshl_add_u32 v249, v248, 4, v249\n\t"
    "v_and_b32 v248, 15, v1\n\t"
    "v_lshlrev_b32 v248, 7, v248\n\t"
    "v_mov_b64 v[250:251], 0\n\t"
    "s_waitcnt lgkmcnt(10)\n\t"
    "buffer_store_dwordx4 v[36:39], v30, s[48:51], s22 offen sc1\n\t"
    "s_add_u32 s22, s22, 67072\n\t"
    "s_waitcnt lgkmcnt(8)\n\t"
    "buffer_store_dwordx4 v[40:43], v30, s[48:51], s22 offen sc1\n\t"
    "s_add_u32 s22, s22, 67072\n\t"
    "s_waitcnt lgkmcnt(6)\n\t"
    "buffer_store_dwordx4 v[44:47], v30, s[48:51], s22 offen sc1\n\t"
    "s_add_u32 s22, s22, 67072\n\t"
    "s_waitcnt lgkmcnt(4)\n\t"
    "buffer_store_dwordx4 v[48:51], v30, s[48:51], s22 offen sc1\n\t"
    "s_add_u32 s88, s88, .Lur61_%=1-.Lur61_%=0\n\t"
    "s_addc_u32 s89, s89, 0\n\t"
    "s_setpc_b64 s[90:91]\n\t"
    ".Lur61_%=1:\n\t"
    "v_mov_b32 v116, 153904\n\t"
    "v_mov_b32 v117, 1\n\t"
    "v_lshlrev_b32 v5, 4, v1\n\t"
    "s_waitcnt lgkmcnt(0)\n\t"
    "s_add_u32 s78, s78, 0x7f4a7c15\n\t"
    "s_addc_u32 s79, s79, 0x9e3779b9\n\t"
    "v_mov_b64 v[18:19], s[78:79]\n\t"
    "s_waitcnt vmcnt(0)\n\t"
    "s_mov_b64 exec, 1\n\t"
    "ds_add_rtn_u32 v117, v116, v117\n\t"
    "s_waitcnt lgkmcnt(0)\n\t"
    "v_cmp_eq_u32 vcc, 4, v117\n\t"
    "s_cbranch_vccz .Lfl92_%=\n\t"
    "s_mov_b64 exec, 255\n\t"
    "buffer_store_dwordx4 v[18:21], v5, s[48:51], s92 offen offset:2048 sc1\n\t"
    ".Lfl92_%=:\n\t"
    "s_mov_b64 exec, -1\n\t"
    "ds_read2_b32 v[36:37], v118 offset0:0 offset1:1\n\t"
    "ds_read2_b32 v[38:39], v118 offset0:2 offset1:3\n\t"
    "ds_read2_b32 v[40:41], v118 offset0:64 offset1:65\n\t"
    "ds_read2_b32 v[42:43], v118 offset0:66 offset1:67\n\t"
    "ds_read2_b32 v[44:45], v118 offset0:128 offset1:129\n\t"
    "ds_read2_b32 v[46:47], v118 offset0:130 offset1:131\n\t"
    "ds_read2_b32 v[48:49], v118 offset0:192 offset1:193\n\t"
    "ds_read2_b32 v[50:51], v118 offset0:194 offset1:195\n\t"
    "s_mul_i32 s23, s13, 16768\n\t"
    "s_lshl_b32 s22, s12, 2\n\t"
    "s_lshl4_add_u32 s23, s22, s23\n\t"
    "s_add_u32 s23, s23, 3330048\n\t"
    "s_lshl_b32 s22, s9, 11\n\t"
    "s_add_u32 s22, s22, 2048\n\t"
    "s_mov_b32 s93, s21\n\t"
    "s_add_u32 s88, s88, .Lur61_%=2-.Lur61_%=1\n\t"
    "s_addc_u32 s89, s89, 0\n\t"
    "s_setpc_b64 s[90:91]\n\t"
    ".Lur61_%=2:\n\t"
    "buffer_load_dwordx2 v[16:17], v248, s[48:51], s22 offen sc1\n\t"
    "s_add_u32 s88, s88, .Lur61_%=3-.Lur61_%=2\n\t"
    "s_addc_u32 s89, s89, 0\n\t"
    "s_setpc_b64 s[90:91]\n\t"
    ".Lur61_%=3:\n\t"
    "s_waitcnt vmcnt(0) lgkmcnt(0)\n\t"
    "v_cmp_ne_u64 vcc, v[18:19], v[16:17]\n\t"
    "s_cbranch_vccz .Lur61_%=4\n\t"
    "buffer_load_dwordx2 v[16:17], v248, s[48:51], s22 offen sc1\n\t"
    "s_setpc_b64 s[90:91]\n\t"
    ".Lur61_%=4:\n\t"
    "ds_write_b64 v31, v[250:251] offset:11616\n\t"
    "s_add_u32 m0, s18, 12128\n\t"
    "s_nop 0\n\t"
    "buffer_load_dwordx4 v249, s[48:51], s23 offen sc1 lds\n\t"
    "s_add_u32 m0, m0, 1024\n\t"
    "s_add_u32 s23, s23, 4096\n\t"
    "buffer_load_dwordx4 v249, s[48:51], s23 offen sc1 lds\n\t"
    "s_add_u32 m0, m0, 1024\n\t"
    "s_add_u32 s23, s23, 4096\n\t"
    "buffer_load_dwordx4 v249, s[48:51], s23 offen sc1 lds\n\t"
    "s_add_u32 m0, m0, 1024\n\t"
    "s_add_u32 s23, s23, 4096\n\t"
    "buffer_load_dwordx4 v249, s[48:51], s23 offen sc1 lds\n\t"
    "s_mov_b32 m0, s18\n\t"
    "s_mov_b32 s22, 0\n\t"
    "s_movk_i32 s93, 99\n\t"
    "s_setpc_b64 s[90:91]\n\t"
    ".LS5_%=:\n\t"
    "s_cmp_ge_u32 s8, 7\n\t"
    "s_cbranch_scc1 .Lq56_%=\n\t"
    "s_bitcmp0_b32 7, s8\n\t"
    "s_cselect_b32 s19, s19, 0x80000000\n\t"
    "buffer_load_dword v252, v119, s[84:87], s19 offen\n\t"
    "s_cmp_lg_u32 s15, 0\n\t"
    "s_cbranch_scc1 .Lq56_%=\n\t"
    "s_cmp_lg_u32 s8, 3\n\t"
    "s_cbranch_scc1 .Lwd93_%=\n\t"
    "s_cmp_lt_u32 %[bid], 255\n\t"
    "s_cbranch_scc1 .Lwd93_%=\n\t"
    "s_load_dwordx2 s[78:79], s[48:49], 0x0\n\t"
    "v_lshlrev_b32 v13, 7, v1\n\t"
    "s_mov_b32 s16, 18432\n\t"
    "s_mov_b64 exec, 0xff\n\t"
    "buffer_load_dword v252, v13, s[48:51], 0 offen offset:128\n\t"
    "s_mov_b64 exec, -1\n\t"
    "s_sleep 70\n\t"
    "s_waitcnt lgkmcnt(0)\n\t"
    "s_add_u32 s78, s78, 0x7f4a7c15\n\t"
    "s_addc_u32 s79, s79, 0x9e3779b9\n\t"
    "v_mov_b64 v[18:19], s[78:79]\n\t"
    "v_and_b32 v14, 12, v1\n\t"
    "v_cmp_eq_u32 vcc, 12, v14\n\t"
    "v_add_u32 v14, 0x2000, v13\n\t"
    "s_nop 0\n\t"
    "v_cndmask_b32 v14, v14, v13, vcc\n\t"
    "v_mov_b32 v12, v14\n\t"
    ".Lf94_%=:\n\t"
    "buffer_load_dwordx3 v[24:26], v13, s[48:51], s16 offen sc1\n\t"
    "buffer_load_dwordx3 v[28:30], v12, s[48:51], s16 offen sc1\n\t"
    "s_add_u32 s35, s35, 16\n\t"
    "s_cmp_lt_u32 s35, 0x400000\n\t"
    "s_cselect_b32 s34, 1, 0\n\t"
    "s_waitcnt vmcnt(0)\n\t"
    "v_cmp_ne_u64 vcc, v[18:19], v[24:25]\n\t"
    "v_cmp_ne_u64 s[86:87], v[18:19], v[28:29]\n\t"
    "s_or_b64 vcc, vcc, s[86:87]\n\t"
    "s_cbranch_vccz .Lg95_%=\n\t"
    "s_sleep 1\n\t"
    "s_cmp_lg_u32 s34, 0\n\t"
    "s_cbranch_scc1 .Lf94_%=\n\t"
    ".Lg95_%=:\n\t"
    "s_memrealtime s[80:81]\n\t"
    "s_waitcnt lgkmcnt(0)\n\t"
    "s_bfe_u32 s83, s80, 196613\n\t"
    "s_lshl_b32 s83, s83, 0\n\t"
    "v_sub_u32 v24, s80, v26\n\t"
    "v_sub_u32 v25, s80, v30\n\t"
    "v_lshrrev_b32 v14, 3, v1\n\t"
    "v_min_u32 v24, v24, v25\n\t"
    "v_add3_u32 v14, v14, s83, 1\n\t"
    "v_and_b32 v15, 7, v1\n\t"
    "v_min_u32_dpp v24, v24, v24 row_ror:8 row_mask:0xf bank_mask:0xf\n\t"
    "s_nop 1\n\t"
    "v_min_u32_dpp v24, v24, v24 row_ror:4 row_mask:0xf bank_mask:0xf\n\t"
    "s_nop 1\n\t"
    "v_min_u32_dpp v24, v24, v24 row_ror:2 row_mask:0xf bank_mask:0xf\n\t"
    "s_nop 1\n\t"
    "v_min_u32_dpp v24, v24, v24 row_ror:1 row_mask:0xf bank_mask:0xf\n\t"
    "v_and_b32 v14, 7, v14\n\t"
    "v_lshlrev_b32 v15, 4, v15\n\t"
    "v_readlane_b32 s20, v24, 0\n\t"
    "v_readlane_b32 s21, v24, 16\n\t"
    "v_readlane_b32 s22, v24, 32\n\t"
    "v_readlane_b32 s23, v24, 48\n\t"
    "v_lshl_add_u32 v14, v14, 7, v15\n\t"
    "s_min_u32 s20, s20, s21\n\t"
    "s_min_u32 s22, s22, s23\n\t"
    "s_min_u32 s20, s20, s22\n\t"
    "s_min_u32 s20, s20, 120\n\t"
    "s_sub_u32 s20, s80, s20\n\t"
    "s_movk_i32 s16, 2048\n\t"
    "s_mov_b32 s21, 51072\n\t"
    "v_mov_b32 v20, s20\n\t"
    "s_mov_b64 exec, 0xffff\n\t"
    "buffer_store_dwordx4 v[18:21], v14, s[48:51], 0 offen offset:128 sc1\n\t"
    "s_not_b64 exec, exec\n\t"
    "buffer_store_dwordx4 v[18:21], v14, s[48:51], 0 offen offset:128 sc1\n\t"
    "s_mov_b64 exec, -1\n\t"
    "s_sleep 70\n\t"
    ".Lf96_%=:\n\t"
    "buffer_load_dwordx2 v[24:25], v13, s[48:51], s16 offen sc1\n\t"
    "s_add_u32 s34, s16, 8192\n\t"
    "buffer_load_dwordx2 v[28:29], v13, s[48:51], s34 offen sc1\n\t"
    "s_add_u32 s34, s16, 24576\n\t"
    "buffer_load_dwordx2 v[32:33], v13, s[48:51], s34 offen sc1\n\t"
    "s_add_u32 s35, s35, 16\n\t"
    "s_cmp_lt_u32 s35, 0x400000\n\t"
    "s_cselect_b32 s34, 1, 0\n\t"
    "s_waitcnt vmcnt(0)\n\t"
    "v_cmp_ne_u64 vcc, v[18:19], v[24:25]\n\t"
    "v_cmp_ne_u64 s[86:87], v[18:19], v[28:29]\n\t"
    "s_or_b64 vcc, vcc, s[86:87]\n\t"
    "v_cmp_ne_u64 s[86:87], v[18:19], v[32:33]\n\t"
    "s_or_b64 vcc, vcc, s[86:87]\n\t"
    "s_cbranch_vccz .Lg97_%=\n\t"
    "s_sleep 8\n\t"
    "s_cmp_lg_u32 s34, 0\n\t"
    "s_cbranch_scc1 .Lf96_%=\n\t"
    ".Lg97_%=:\n\t"
    "s_mov_b64 exec, 1\n\t"
    "buffer_store_dwordx2 v[18:19], v13, s[48:51], 0 offen sc1\n\t"
    "s_mov_b64 exec, -1\n\t"
    "s_endpgm\n\t"
    ".Ls258_%=:\n\t"
    "s_cmp_lt_u32 s8, 6\n\t"
    "s_cbranch_scc0 .Lwd93_%=f\n\t"
    "buffer_load_dword v252, v22, s[40:43], s28 offen\n\t"
    "s_sleep 10\n\t"
    "buffer_load_dword v252, v23, s[44:47], s29 offen\n\t"
    "buffer_load_dword v252, v23, s[44:47], s27 offen\n\t"
    ".Lwd93_%=f:\n\t"
    "s_cmp_eq_u32 s8, 7\n\t"
    "s_cbranch_scc0 .Lwd93_%=\n\t"
    "s_load_dwordx2 s[78:79], s[48:49], 0x0\n\t"
    "s_lshl_b32 s92, s13, 7\n\t"
    "v_mov_b32 v116, 153904\n\t"
    "v_mov_b32 v117, 1\n\t"
    "v_lshlrev_b32 v5, 4, v1\n\t"
    "s_waitcnt lgkmcnt(0)\n\t"
    "s_add_u32 s78, s78, 0x7f4a7c15\n\t"
    "s_addc_u32 s79, s79, 0x9e3779b9\n\t"
    "v_mov_b64 v[18:19], s[78:79]\n\t"
    "s_waitcnt vmcnt(0)\n\t"
    "s_mov_b64 exec, 255\n\t"
    "buffer_store_dwordx4 v[18:21], v5, s[48:51], s92 offen offset:2048 sc1\n\t"
    ".Lfl98_%=:\n\t"
    "s_mov_b64 exec, -1\n\t"
    "s_sleep 100\n\t"
    "s_memtime s[60:61]\n\t"
    "s_memrealtime s[62:63]\n\t"
    "s_branch .Lrd81_%=\n\t"
    ".Lwd93_%=:\n\t"
    "s_endpgm\n\t"
    ".LD7_%=:\n\t"
    : "=&{s[36:51]}"(k0), "=&{s[68:83]}"(k2), "=&{v255}"(stv), "=&{v254}"(trv)
    : [ka] "s"(__builtin_amdgcn_kernarg_segment_ptr()), [bid] "s"(b), [tid] "v"(tid), [sm] "s"((u32)(size_t)(__attribute__((address_space(3))) char*)smem)
    : "memory", "scc", "vcc", "v1", "v2", "v3", "v4", "v5", "v6", "v7", "v8", "v9", "v10", "v11", "v12", "v13", "v14", "v15", "v16", "v17", "v18", "v19", "v20", "v21", "v22", "v23", "v24", "v25", "v26", "v27", "v28", "v29", "v30", "v31", "v32", "v33", "v34", "v35", "v36", "v37", "v38", "v39", "v40", "v41", "v42", "v43", "v44", "v45", "v46", "v47", "v48", "v49", "v50", "v51", "v52", "v53", "v54", "v55", "v56", "v57", "v58", "v59", "v60", "v61", "v62", "v63", "v64", "v65", "v66", "v67", "v68", "v69", "v70", "v71", "v72", "v73", "v74", "v75", "v76", "v77", "v78", "v79", "v80", "v81", "v82", "v83", "v84", "v85", "v86", "v87", "v88", "v89", "v90", "v91", "v92", "v93", "v94", "v95", "v96", "v97", "v98", "v99", "v100", "v101", "v102", "v103", "v104", "v105", "v106", "v107", "v108", "v109", "v110", "v111", "v112", "v113", "v114", "v115", "v116", "v117", "v118", "v119", "v120", "v121", "v122", "v123", "v124", "v125", "v126", "v127", "v128", "v129", "v130", "v131", "v132", "v133", "v134", "v135", "v136", "v137", "v138", "v139", "v140", "v141", "v142", "v143", "v144", "v145", "v146", "v147", "v148", "v149", "v150", "v151", "v152", "v153", "v154", "v155", "v156", "v157", "v158", "v159", "v160", "v161", "v162", "v163", "v164", "v165", "v166", "v167", "v168", "v169", "v170", "v171", "v172", "v173", "v174", "v175", "v176", "v177", "v178", "v179", "v180", "v181", "v182", "v183", "v184", "v185", "v186", "v187", "v188", "v189", "v190", "v191", "v192", "v193", "v194", "v195", "v196", "v197", "v198", "v199", "v200", "v201", "v202", "v203", "v204", "v205", "v206", "v207", "v208", "v209", "v210", "v211", "v212", "v213", "v214", "v215", "v216", "v217", "v218", "v219", "v220", "v221", "v222", "v223", "v224", "v225", "v226", "v227", "v228", "v229", "v230", "v231", "v232", "v233", "v234", "v235", "v236", "v237", "v238", "v239", "v240", "v241", "v242", "v243", "v244", "v245", "v246", "v247", "v248", "v249", "v250", "v251", "v252", "v253", "s8", "s9", "s10", "s11", "s12", "s13", "s14", "s15", "s16", "s17", "s18", "s19", "s20", "s21", "s22", "s23", "s24", "s25", "s26", "s27", "s28", "s29", "s30", "s31", "s34", "s35", "s52", "s53", "s54", "s55", "s56", "s57", "s58", "s59", "s60", "s61", "s62", "s63", "s64", "s65", "s66", "s67", "s84", "s85", "s86", "s87", "s88", "s89", "s90", "s91", "s92", "s93", "s94", "s95", "s96", "s97", "s98", "s99");
}

extern "C" void kernel_launch(void* const* d_in, const int* in_sizes, int n_in, void* d_out, int out_size, void* d_ws, size_t ws_size,
                              hipStream_t stream) {
    const u64 raw = 0x00020000ull << 32;
    Params P;
    P.x = {d_in[9], raw | 2048 * 896}; P.wqkv = {d_in[10], raw | 448 * 2688}; P.wo = {d_in[11], raw | 448 * 896};
    P.ws = {d_ws, raw | 5208064}; P.out = {d_out, raw | 2048 * 1792}; P.u = {d_in[0], raw | 2048 * 896};
    P.ldt = d_in[7]; P.Dsk = d_in[8];
    P.Are = d_in[1]; P.Aim = d_in[2]; P.Bre = d_in[3]; P.Bim = d_in[4]; P.Cre = d_in[5]; P.Cim = d_in[6];
    P.slot = 0; P.zero = 0;
    fused_kernel<<<256, 512, 0, stream>>>(P);
}
```
